```python
import torch

def _softmax(x, axis=-1):
    return torch.softmax(x, dim=axis)
def _gelu(x):
    return torch.nn.functional.gelu(x, approximate="tanh")
def _silu(x):
    return torch.nn.functional.silu(x)
def _sigmoid(x):
    return torch.sigmoid(x)
def _layernorm(x, axis=-1, eps=1e-5):
    m = x.mean(dim=axis, keepdim=True)
    v = x.var(dim=axis, keepdim=True, unbiased=False)
    return (x - m) / torch.sqrt(v + eps)
def _scatter_add(tgt, idx, src, axis=0):
    return tgt.clone().index_add_(axis, idx, src)
def _segment_sum(src, seg, n):
    out = torch.zeros((n,) + src.shape[1:], dtype=src.dtype)
    return out.index_add_(0, seg, src)
def _pad(x, pw):
    flat = tuple(v for pair in reversed(pw) for v in pair)
    return torch.nn.functional.pad(x, flat)

kernel_name = 'standalone__gqa_block_cached__near_ridge'

def setup_inputs():

    _w = (lambda a: (lambda t: t.to(torch.bfloat16) if t.is_floating_point() else t)(torch.as_tensor(a)))
    B, Sq, D = 16, 96, 448
    Hq, Hkv, Sk, Dh = 16, 8, 1024, 256
    assert Hq % Hkv == 0, (Hq, Hkv)
    return {
        "x": _w((lambda *s: torch.randn(*s, dtype=torch.bfloat16) * 0.8)(B, Sq, D)),
        "k_cache": _w((lambda *s: torch.randn(*s, dtype=torch.bfloat16) * 0.8)(B, Hkv, Sk, Dh)),
        "v_cache": _w((lambda *s: torch.randn(*s, dtype=torch.bfloat16) * 0.05)(B, Hkv, Sk, Dh)),
        "wq": _w((lambda *s: torch.randn(*s, dtype=torch.bfloat16) * 0.8)(D, Hq * Dh) * (D ** -0.5)),
        "wo": _w((lambda *s: torch.randn(*s, dtype=torch.bfloat16) * 0.05)(Hq * Dh, D) * ((Hq * Dh) ** -0.5)),
    }

def reference(x, k_cache, v_cache, wq, wo):
    B, Sq, D = x.shape
    Hq, Dh = 16, 256
    Hkv = k_cache.shape[1]
    rep = Hq // Hkv
    q = (lambda x, *d: x.permute(*d))((x @ wq).reshape(B, Sq, Hq, Dh), 0, 2, 1, 3)
    k = (lambda x, n, axis: torch.repeat_interleave(x, n, dim=axis))(k_cache, rep, axis=1)
    v = (lambda x, n, axis: torch.repeat_interleave(x, n, dim=axis))(v_cache, rep, axis=1)
    scores = torch.einsum("bhqd,bhkd->bhqk", q, k) * (Dh ** -0.5)
    p = _softmax(scores, axis=-1)
    o = torch.einsum("bhqk,bhkd->bhqd", p, v)
    return (lambda x, *d: x.permute(*d))(o, 0, 2, 1, 3).reshape(B, Sq, Hq * Dh) @ wo
```

```cpp
#include <hip/hip_runtime.h>
#include <hip/hip_bf16.h>
typedef unsigned short u16;
__device__ inline float b2f(u16 v){ return __uint_as_float(((unsigned)v)<<16); }
__device__ inline u16 f2b(float f){ unsigned u=__float_as_uint(f); u += 0x7fff + ((u>>16)&1); return (u16)(u>>16); }
constexpr int B=16,SQ=96,D=448,HQ=16,HKV=8,SK=1024,DH=256;
__global__ void gemm_naive(const u16* A,const u16* W,u16* C,int M,int N,int K){
  int n = blockIdx.x*64 + (threadIdx.x&63); int m = blockIdx.y*4 + (threadIdx.x>>6);
  if(n>=N||m>=M) return;
  float acc=0; for(int k=0;k<K;k++) acc += b2f(A[(size_t)m*K+k])*b2f(W[(size_t)k*N+n]);
  C[(size_t)m*N+n]=f2b(acc);
}
__global__ void attn_naive(const u16* Q,const u16* Kc,const u16* Vc,u16* O){
  int q=blockIdx.x, h=blockIdx.y, b=blockIdx.z, t=threadIdx.x;
  __shared__ float qs[DH]; __shared__ float s[SK]; __shared__ float red[256];
  qs[t]=b2f(Q[((size_t)(b*SQ+q))*HQ*DH + h*DH + t]);
  __syncthreads();
  const u16* Kp=Kc+((size_t)(b*HKV+h/2))*SK*DH; const u16* Vp=Vc+((size_t)(b*HKV+h/2))*SK*DH;
  float mx=-1e30f;
  for(int k=t;k<SK;k+=256){ float a=0; for(int d=0;d<DH;d++) a+=qs[d]*b2f(Kp[(size_t)k*DH+d]); a*=0.0625f; s[k]=a; mx=fmaxf(mx,a);}
  red[t]=mx; __syncthreads();
  for(int o=128;o>0;o>>=1){ if(t<o) red[t]=fmaxf(red[t],red[t+o]); __syncthreads(); }
  mx=red[0]; __syncthreads();
  float sum=0; for(int k=t;k<SK;k+=256){ float p=__expf(s[k]-mx); s[k]=p; sum+=p; }
  red[t]=sum; __syncthreads();
  for(int o=128;o>0;o>>=1){ if(t<o) red[t]+=red[t+o]; __syncthreads(); }
  sum=red[0];
  float acc=0; for(int k=0;k<SK;k++) acc+=s[k]*b2f(Vp[(size_t)k*DH+t]);
  O[((size_t)(b*SQ+q))*HQ*DH + h*DH + t]=f2b(acc/sum);
}
extern "C" void kernel_launch(void* const* d_in, const int* in_sizes, int n_in, void* d_out, int out_size, void* d_ws, size_t ws_size, hipStream_t stream){
  const u16* x=(const u16*)d_in[0]; const u16* kc=(const u16*)d_in[1]; const u16* vc=(const u16*)d_in[2];
  const u16* wq=(const u16*)d_in[3]; const u16* wo=(const u16*)d_in[4];
  u16* Q=(u16*)d_ws; u16* O=Q+(size_t)B*SQ*HQ*DH;
  gemm_naive<<<dim3(HQ*DH/64,B*SQ/4),256,0,stream>>>(x,wq,Q,B*SQ,HQ*DH,D);
  attn_naive<<<dim3(SQ,HQ,B),256,0,stream>>>(Q,kc,vc,O);
  gemm_naive<<<dim3(D/64,B*SQ/4),256,0,stream>>>(O,wo,(u16*)d_out,B*SQ,D,HQ*DH);
}
```

```cpp
#include <hip/hip_runtime.h>
#include <hip/hip_bf16.h>

constexpr int LDS_BYTES = 163840;
constexpr int PART_BYTES = 86016;
constexpr size_t FLAG_OFF = (size_t)32 << 20;

__global__ __launch_bounds__(256, 1) void gqa_main(const char* __restrict__ x, const char* __restrict__ kc, const char* __restrict__ vc,
                                                   const char* __restrict__ wq, const char* __restrict__ wo, char* ws, char* out) {
  const unsigned blk = blockIdx.x, kvh = blk & 7u, j = blk >> 3, b = j >> 1, h = 2u * kvh + (j & 1u);
  const unsigned wid = __builtin_amdgcn_readfirstlane(threadIdx.x >> 6);
  const char* px = x + (size_t)b * 86016u;
  const char* pk = kc + (size_t)(b * 8u + kvh) * 524288u;
  const char* pv = vc + (size_t)(b * 8u + kvh) * 524288u;
  const char* pwq = wq + (size_t)h * 512u;
  const unsigned wqsz = 448u * 8192u - h * 512u;
  const unsigned rot = 4u * ((b + kvh + 2u * (j & 1u)) & 3u);
  const char* pwo = wo + (size_t)h * 229376u;
  char* pp = ws + (size_t)b * 16u * PART_BYTES;
  char* pf = ws + FLAG_OFF + (size_t)b * 512u;
  char* pout = out + (size_t)b * 86016u;
  asm volatile(
    "v_mbcnt_lo_u32_b32 v254, -1, 0\n"
    "v_mbcnt_hi_u32_b32 v254, -1, v254\n"
    "s_mov_b32 s64, %[wid]\n"
    "v_lshrrev_b32 v172, 5, v254\n"
    "v_and_b32 v175, 31, v254\n"
    "s_lshl_b32 s65, s64, 13\n"
    "s_mov_b64 s[44:45], %[pwq]\n"
    "s_and_b32 s45, s45, 0xffff\n"
    "s_mov_b32 s46, %[wqsz]\n"
    "s_mov_b32 s47, 0x20000\n"
    "s_and_b32 s77, s64, 1\n"
    "s_lshl_b32 s76, s77, 3\n"
    "v_lshlrev_b32 v176, 1, v172\n"
    "v_xor_b32 v176, v175, v176\n"
    "v_xor_b32 v176, s76, v176\n"
    "v_lshlrev_b32 v176, 4, v176\n"
    "v_lshl_or_b32 v144, v172, 13, v176\n"
    "v_xor_b32 v177, 64, v144\n"
    "v_add_u32 v145, 15360, v177\n"
    "v_add_u32 v146, 30720, v144\n"
    "v_add_u32 v147, 46080, v177\n"
    "s_lshl_b32 s67, s64, 16\n"
    "s_lshl_b32 s71, %[rot], 18\n"
    "s_add_u32 s71, s71, s67\n"
    "s_lshl_b32 s78, s64, 12\n"
    "s_mov_b32 s69, s78\n"
    "s_add_u32 m0, s69, 0\n"
    "s_nop 0\n"
    "buffer_load_dwordx4 v144, s[44:47], s71 offen offset:0 lds\n"
    "buffer_load_dwordx4 v145, s[44:47], s71 offen offset:1024 lds\n"
    "buffer_load_dwordx4 v146, s[44:47], s71 offen offset:2048 lds\n"
    "buffer_load_dwordx4 v147, s[44:47], s71 offen offset:3072 lds\n"
    "s_add_u32 s71, s71, 262144\n"
    "s_cmp_ge_u32 s71, 0x380000\n"
    "s_cselect_b32 s71, s67, s71\n"
    "s_mov_b64 s[40:41], %[px]\n"
    "s_and_b32 s41, s41, 0xffff\n"
    "s_mov_b32 s42, 86016\n"
    "s_mov_b32 s43, 0x20000\n"
    "s_mul_i32 s76, s64, 24\n"
    "v_lshrrev_b32 v176, 3, v254\n"
    "v_add_u32 v176, s76, v176\n"
    "v_mul_u32_u24 v176, 896, v176\n"
    "v_lshrrev_b32 v177, 4, v254\n"
    "v_xor_b32 v177, v177, v254\n"
    "s_lshl_b32 s77, s77, 2\n"
    "v_xor_b32 v177, s77, v177\n"
    "v_and_b32 v177, 7, v177\n"
    "v_lshl_add_u32 v179, v177, 4, v176\n"
    "v_add_u32 v181, 12288, v179\n"
    "v_xor_b32 v177, 4, v177\n"
    "v_lshl_add_u32 v177, v177, 4, v176\n"
    "v_add_u32 v180, 6144, v177\n"
    "s_mul_i32 s68, s64, 3072\n"
    "s_lshl_b32 s95, %[rot], 6\n"
    "s_add_u32 m0, s68, 114688\n"
    "s_nop 0\n"
    "buffer_load_dwordx4 v179, s[40:43], s95 offen offset:0 lds\n"
    "buffer_load_dwordx4 v180, s[40:43], s95 offen offset:1024 lds\n"
    "buffer_load_dwordx4 v181, s[40:43], s95 offen offset:2048 lds\n"
    "s_add_u32 s95, s95, 128\n"
    "s_cmp_eq_u32 s95, 0x380\n"
    "s_cselect_b32 s95, 0, s95\n"
    "s_mov_b32 s36, %[bidx]\n"
    "s_mov_b32 s37, %[hidx]\n"
    "v_lshlrev_b32 v176, 7, v254\n"
    "s_add_u32 m0, s69, 16384\n"
    "s_nop 0\n"
    "buffer_load_dwordx4 v144, s[44:47], s71 offen offset:0 lds\n"
    "buffer_load_dwordx4 v145, s[44:47], s71 offen offset:1024 lds\n"
    "buffer_load_dwordx4 v146, s[44:47], s71 offen offset:2048 lds\n"
    "buffer_load_dwordx4 v147, s[44:47], s71 offen offset:3072 lds\n"
    "s_add_u32 s71, s71, 262144\n"
    "s_cmp_ge_u32 s71, 0x380000\n"
    "s_cselect_b32 s71, s67, s71\n"
    "s_mov_b64 s[48:49], %[pk]\n"
    "s_and_b32 s49, s49, 0xffff\n"
    "s_mov_b32 s50, 524288\n"
    "s_mov_b32 s51, 0x20000\n"
    "s_mov_b64 s[52:53], %[pv]\n"
    "s_and_b32 s53, s53, 0xffff\n"
    "s_mov_b32 s54, 524288\n"
    "s_mov_b32 s55, 0x20000\n"
    "s_mov_b64 s[56:57], %[pwo]\n"
    "s_and_b32 s57, s57, 0xffff\n"
    "s_mov_b32 s58, 229376\n"
    "s_mov_b32 s59, 0x20000\n"
    "s_mov_b64 s[60:61], %[pp]\n"
    "s_and_b32 s61, s61, 0xffff\n"
    "s_mov_b32 s62, 1376256\n"
    "s_mov_b32 s63, 0x20000\n"
    "v_and_b32 v170, 15, v254\n"
    "v_lshrrev_b32 v171, 4, v254\n"
    "v_bfe_u32 v173, v254, 2, 2\n"
    "v_and_b32 v174, 3, v254\n"
    "s_mul_i32 s91, s64, 2048\n"
    "s_mul_i32 s76, s64, 4\n"
    "v_xor_b32 v176, v175, v172\n"
    "v_xor_b32 v176, s76, v176\n"
    "v_lshlrev_b32 v176, 4, v176\n"
    "v_lshl_or_b32 v200, v172, 9, v176\n"
    "v_xor_b32 v201, 32, v200\n"
    "v_xor_b32 v202, 8192, v200\n"
    "v_xor_b32 v203, 8224, v200\n"
    "v_xor_b32 v204, 16384, v200\n"
    "v_xor_b32 v205, 16416, v200\n"
    "v_xor_b32 v206, 24576, v200\n"
    "v_xor_b32 v207, 24608, v200\n"
    "s_lshr_b32 s76, s64, 1\n"
    "s_and_b32 s77, s64, 1\n"
    "v_bfe_u32 v176, v254, 2, 3\n"
    "v_lshl_add_u32 v177, v171, 3, v173\n"
    "v_lshlrev_b32 v177, 9, v177\n"
    "v_lshl_add_u32 v177, v174, 3, v177\n"
    "s_lshl_b32 s78, s64, 2\n"
    "v_xor_b32 v178, s78, v176\n"
    "v_lshl_add_u32 v150, v178, 5, v177\n"
    "v_xor_b32 v151, 32, v150\n"
    "v_xor_b32 v152, 64, v150\n"
    "v_xor_b32 v153, 96, v150\n"
    "v_add_u32 v154, 65536, v150\n"
    "v_add_u32 v155, 65536, v151\n"
    "v_add_u32 v156, 65536, v152\n"
    "v_add_u32 v157, 65536, v153\n"
    "v_lshrrev_b32 v178, 1, v170\n"
    "v_xor_b32 v178, v178, v171\n"
    "v_lshlrev_b32 v178, 4, v178\n"
    "v_lshl_add_u32 v178, v170, 7, v178\n"
    "v_add_u32 v158, 114688, v178\n"
    "v_xor_b32 v159, 64, v158\n"
    "v_lshrrev_b32 v177, 2, v254\n"
    "v_lshlrev_b32 v177, 9, v177\n"
    "v_lshl_add_u32 v177, v174, 3, v177\n"
    "s_lshl_b32 s78, s76, 14\n"
    "s_lshl_b32 s79, s76, 8\n"
    "s_add_u32 s79, s79, 65536\n"
    "v_add_u32 v177, s79, v177\n"
    "v_lshl_add_u32 v192, v176, 5, v177\n"
    "v_xor_b32 v193, 32, v192\n"
    "v_xor_b32 v194, 64, v192\n"
    "v_xor_b32 v195, 96, v192\n"
    "v_xor_b32 v196, 128, v192\n"
    "v_xor_b32 v197, 160, v192\n"
    "v_xor_b32 v198, 192, v192\n"
    "v_xor_b32 v199, 224, v192\n"
    "v_xor_b32 v178, v171, v170\n"
    "v_lshlrev_b32 v178, 4, v178\n"
    "v_lshl_add_u32 v178, v170, 9, v178\n"
    "v_add_u32 v188, s78, v178\n"
    "v_xor_b32 v189, 64, v188\n"
    "v_xor_b32 v190, 128, v188\n"
    "v_xor_b32 v191, 192, v188\n"
    "s_mul_i32 s79, s77, 24576\n"
    "s_add_u32 s79, s79, 114688\n"
    "v_add_u32 v166, s79, v178\n"
    "v_xor_b32 v167, 64, v166\n"
    "v_xor_b32 v168, 128, v166\n"
    "v_xor_b32 v169, 192, v166\n"
    "v_lshrrev_b32 v177, 1, v171\n"
    "s_lshl_b32 s79, s64, 3\n"
    "v_add_u32 v177, s79, v177\n"
    "v_xor_b32 v177, v177, v170\n"
    "v_lshlrev_b32 v177, 4, v177\n"
    "v_lshl_add_u32 v177, v170, 9, v177\n"
    "v_and_b32 v178, 1, v171\n"
    "v_lshl_add_u32 v177, v178, 3, v177\n"
    "v_add_u32 v162, 114688, v177\n"
    "v_xor_b32 v163, 32, v162\n"
    "v_xor_b32 v164, 64, v162\n"
    "v_xor_b32 v165, 96, v162\n"
    "s_mul_i32 s79, s64, 8\n"
    "s_and_b32 s79, s79, 14\n"
    "v_lshlrev_b32 v177, 1, v172\n"
    "v_xor_b32 v177, v175, v177\n"
    "v_xor_b32 v177, s79, v177\n"
    "v_lshlrev_b32 v177, 4, v177\n"
    "v_lshl_or_b32 v208, v172, 9, v177\n"
    "v_xor_b32 v209, 64, v208\n"
    "v_xor_b32 v210, 8192, v208\n"
    "v_xor_b32 v211, 8256, v208\n"
    "v_bfe_u32 v177, v254, 1, 2\n"
    "v_xor_b32 v177, v177, v171\n"
    "v_lshlrev_b32 v177, 5, v177\n"
    "v_and_b32 v178, 1, v254\n"
    "v_lshl_add_u32 v177, v178, 4, v177\n"
    "v_lshrrev_b32 v178, 3, v254\n"
    "v_mul_u32_u24 v178, 896, v178\n"
    "v_add_u32 v240, v177, v178\n"
    "v_add_u32 v241, 6144, v240\n"
    "v_add_u32 v242, 12288, v240\n"
    "v_add_u32 v243, 18432, v240\n"
    "v_add_u32 v244, 28672, v240\n"
    "v_add_u32 v245, 34816, v240\n"
    "v_add_u32 v246, 40960, v240\n"
    "v_add_u32 v247, 47104, v240\n"
    "v_lshrrev_b32 v177, 2, v170\n"
    "v_cmp_eq_u32 vcc, v177, v171\n"
    "v_and_b32 v178, 1, v170\n"
    "v_lshlrev_b32 v178, 4, v178\n"
    "v_mov_b32 v224, 0\n"
    "v_mov_b32 v225, 0x3f80\n"
    "v_lshlrev_b32 v178, v178, v225\n"
    "v_cndmask_b32 v178, v224, v178, vcc\n"
    "v_and_b32 v177, 2, v170\n"
    "v_cmp_eq_u32 vcc, 0, v177\n"
    "v_mov_b32 v225, 0\n"
    "v_mov_b32 v228, 0\n"
    "v_mov_b32 v229, 0\n"
    "v_cndmask_b32 v226, v224, v178, vcc\n"
    "v_cndmask_b32 v227, v178, v224, vcc\n"
    "v_bfe_u32 v177, v254, 3, 2\n"
    "s_and_b32 s79, s64, 2\n"
    "v_xor_b32 v177, s79, v177\n"
    "v_lshrrev_b32 v178, 2, v254\n"
    "v_lshlrev_b32 v178, 7, v178\n"
    "v_lshl_add_u32 v178, v174, 3, v178\n"
    "v_lshl_add_u32 v177, v177, 5, v178\n"
    "s_lshl_b32 s79, s79, 13\n"
    "v_add_u32 v223, s79, v177\n"
    "v_lshlrev_b32 v250, 4, v254\n"
    "v_lshlrev_b32 v253, 2, v170\n"
    "s_mul_i32 s79, s64, 12288\n"
    "s_add_u32 s79, s79, 32768\n"
    "v_add_u32 v249, s79, v250\n"
    "s_xor_b32 s78, s64, 2\n"
    "s_mul_i32 s79, s78, 12288\n"
    "s_add_u32 s79, s79, 32768\n"
    "v_add_u32 v248, s79, v250\n"
    "s_lshl_b32 s79, s64, 8\n"
    "s_add_u32 s79, s79, 157696\n"
    "v_add_u32 v251, s79, v253\n"
    "s_lshl_b32 s79, s78, 8\n"
    "s_add_u32 s79, s79, 157696\n"
    "v_add_u32 v252, s79, v253\n"
    "s_mov_b32 s66, s91\n"
    "s_mov_b32 s70, 0x45800000\n"
    "s_mov_b32 s72, 0x3db8aa3b\n"
    "s_mov_b32 s73, 0x3db8aa3b\n"
    "v_mov_b32 v212, 0\n"
    "v_mov_b32 v213, 0\n"
    "v_mov_b32 v214, 0\n"
    "s_mov_b64 s[76:77], %[pf]\n"
    "s_and_b32 s77, s77, 0xffff\n"
    "s_mov_b32 s78, 256\n"
    "s_mov_b32 s79, 0x20000\n"
    "v_mov_b32 v176, 0\n"
    "buffer_load_dwordx2 v[230:231], v176, s[76:79], 0 offen offset:0 sc1\n"
    "v_mov_b32 v176, 0\n"
    "buffer_load_dword v255, v176, s[48:51], 0 offen offset:0 nt\n"
    "buffer_load_dword v255, v176, s[52:55], 0 offen offset:0 nt\n"
    "s_add_u32 m0, s68, 126976\n"
    "s_nop 0\n"
    "buffer_load_dwordx4 v179, s[40:43], s95 offen offset:0 lds\n"
    "buffer_load_dwordx4 v180, s[40:43], s95 offen offset:1024 lds\n"
    "buffer_load_dwordx4 v181, s[40:43], s95 offen offset:2048 lds\n"
    "s_add_u32 s95, s95, 128\n"
    "s_cmp_eq_u32 s95, 0x380\n"
    "s_cselect_b32 s95, 0, s95\n"
    "s_add_u32 m0, s69, 32768\n"
    "s_nop 0\n"
    "buffer_load_dwordx4 v144, s[44:47], s71 offen offset:0 lds\n"
    "buffer_load_dwordx4 v145, s[44:47], s71 offen offset:1024 lds\n"
    "buffer_load_dwordx4 v146, s[44:47], s71 offen offset:2048 lds\n"
    "buffer_load_dwordx4 v147, s[44:47], s71 offen offset:3072 lds\n"
    "s_add_u32 s71, s71, 262144\n"
    "s_cmp_ge_u32 s71, 0x380000\n"
    "s_cselect_b32 s71, s67, s71\n"
    "s_waitcnt vmcnt(14)\n"
    "s_barrier\n"
    "ds_read_b64_tr_b16 a[0:1], v150 offset:0\n"
    "ds_read_b64_tr_b16 a[2:3], v150 offset:2048\n"
    "ds_read_b128 a[32:35], v158 offset:0\n"
    "ds_read_b128 a[36:39], v158 offset:2048\n"
    "ds_read_b128 a[40:43], v158 offset:4096\n"
    "ds_read_b128 a[44:47], v158 offset:6144\n"
    "ds_read_b128 a[48:51], v158 offset:8192\n"
    "ds_read_b128 a[52:55], v158 offset:10240\n"
    "ds_read_b64_tr_b16 a[4:5], v151 offset:0\n"
    "ds_read_b64_tr_b16 a[6:7], v151 offset:2048\n"
    "ds_read_b64_tr_b16 a[8:9], v152 offset:0\n"
    "ds_read_b64_tr_b16 a[10:11], v152 offset:2048\n"
    "ds_read_b64_tr_b16 a[12:13], v153 offset:0\n"
    "ds_read_b64_tr_b16 a[14:15], v153 offset:2048\n"
    "s_waitcnt lgkmcnt(6)\n"
    "v_mfma_f32_16x16x32_bf16 v[0:3], a[0:3], a[32:35], 0\n"
    "s_add_u32 m0, s69, 49152\n"
    "s_nop 0\n"
    "buffer_load_dwordx4 v144, s[44:47], s71 offen offset:0 lds\n"
    "v_mfma_f32_16x16x32_bf16 v[4:7], a[0:3], a[36:39], 0\n"
    "v_mfma_f32_16x16x32_bf16 v[8:11], a[0:3], a[40:43], 0\n"
    "v_mfma_f32_16x16x32_bf16 v[12:15], a[0:3], a[44:47], 0\n"
    "v_mfma_f32_16x16x32_bf16 v[16:19], a[0:3], a[48:51], 0\n"
    "buffer_load_dwordx4 v145, s[44:47], s71 offen offset:1024 lds\n"
    "v_mfma_f32_16x16x32_bf16 v[20:23], a[0:3], a[52:55], 0\n"
    "s_waitcnt vmcnt(12) lgkmcnt(0)\n"
    "s_barrier\n"
    "v_mfma_f32_16x16x32_bf16 v[24:27], a[4:7], a[32:35], 0\n"
    "ds_read_b64_tr_b16 a[16:17], v150 offset:16384\n"
    "ds_read_b64_tr_b16 a[18:19], v150 offset:18432\n"
    "v_mfma_f32_16x16x32_bf16 v[28:31], a[4:7], a[36:39], 0\n"
    "ds_read_b128 a[56:59], v159 offset:0\n"
    "ds_read_b128 a[60:63], v159 offset:2048\n"
    "v_mfma_f32_16x16x32_bf16 v[32:35], a[4:7], a[40:43], 0\n"
    "buffer_load_dwordx4 v146, s[44:47], s71 offen offset:2048 lds\n"
    "ds_read_b128 a[64:67], v159 offset:4096\n"
    "v_mfma_f32_16x16x32_bf16 v[36:39], a[4:7], a[44:47], 0\n"
    "ds_read_b128 a[68:71], v159 offset:6144\n"
    "ds_read_b128 a[72:75], v159 offset:8192\n"
    "v_mfma_f32_16x16x32_bf16 v[40:43], a[4:7], a[48:51], 0\n"
    "ds_read_b128 a[76:79], v159 offset:10240\n"
    "ds_read_b64_tr_b16 a[20:21], v151 offset:16384\n"
    "v_mfma_f32_16x16x32_bf16 v[44:47], a[4:7], a[52:55], 0\n"
    "ds_read_b64_tr_b16 a[22:23], v151 offset:18432\n"
    "ds_read_b64_tr_b16 a[24:25], v152 offset:16384\n"
    "v_mfma_f32_16x16x32_bf16 v[48:51], a[8:11], a[32:35], 0\n"
    "buffer_load_dwordx4 v147, s[44:47], s71 offen offset:3072 lds\n"
    "s_add_u32 s71, s71, 262144\n"
    "v_mfma_f32_16x16x32_bf16 v[52:55], a[8:11], a[36:39], 0\n"
    "s_cmp_ge_u32 s71, 0x380000\n"
    "s_cselect_b32 s71, s67, s71\n"
    "v_mfma_f32_16x16x32_bf16 v[56:59], a[8:11], a[40:43], 0\n"
    "s_add_u32 m0, s68, 139264\n"
    "ds_read_b64_tr_b16 a[26:27], v152 offset:18432\n"
    "v_mfma_f32_16x16x32_bf16 v[60:63], a[8:11], a[44:47], 0\n"
    "ds_read_b64_tr_b16 a[28:29], v153 offset:16384\n"
    "ds_read_b64_tr_b16 a[30:31], v153 offset:18432\n"
    "v_mfma_f32_16x16x32_bf16 v[64:67], a[8:11], a[48:51], 0\n"
    "buffer_load_dwordx4 v179, s[40:43], s95 offen offset:0 lds\n"
    "v_mfma_f32_16x16x32_bf16 v[68:71], a[8:11], a[52:55], 0\n"
    "v_mfma_f32_16x16x32_bf16 v[72:75], a[12:15], a[32:35], 0\n"
    "v_mfma_f32_16x16x32_bf16 v[76:79], a[12:15], a[36:39], 0\n"
    "v_mfma_f32_16x16x32_bf16 v[80:83], a[12:15], a[40:43], 0\n"
    "buffer_load_dwordx4 v180, s[40:43], s95 offen offset:1024 lds\n"
    "v_mfma_f32_16x16x32_bf16 v[84:87], a[12:15], a[44:47], 0\n"
    "v_mfma_f32_16x16x32_bf16 v[88:91], a[12:15], a[48:51], 0\n"
    "v_mfma_f32_16x16x32_bf16 v[92:95], a[12:15], a[52:55], 0\n"
    "s_waitcnt lgkmcnt(6)\n"
    "v_mfma_f32_16x16x32_bf16 v[0:3], a[16:19], a[56:59], v[0:3]\n"
    "buffer_load_dwordx4 v181, s[40:43], s95 offen offset:2048 lds\n"
    "s_add_u32 s95, s95, 128\n"
    "v_mfma_f32_16x16x32_bf16 v[4:7], a[16:19], a[60:63], v[4:7]\n"
    "s_cmp_eq_u32 s95, 0x380\n"
    "s_cselect_b32 s95, 0, s95\n"
    "v_mfma_f32_16x16x32_bf16 v[8:11], a[16:19], a[64:67], v[8:11]\n"
    "s_add_u32 m0, s69, 65536\n"
    "v_mfma_f32_16x16x32_bf16 v[12:15], a[16:19], a[68:71], v[12:15]\n"
    "v_mfma_f32_16x16x32_bf16 v[16:19], a[16:19], a[72:75], v[16:19]\n"
    "buffer_load_dwordx4 v144, s[44:47], s71 offen offset:0 lds\n"
    "v_mfma_f32_16x16x32_bf16 v[20:23], a[16:19], a[76:79], v[20:23]\n"
    "s_waitcnt vmcnt(8) lgkmcnt(0)\n"
    "s_barrier\n"
    "v_mfma_f32_16x16x32_bf16 v[24:27], a[20:23], a[56:59], v[24:27]\n"
    "ds_read_b64_tr_b16 a[0:1], v150 offset:32768\n"
    "ds_read_b64_tr_b16 a[2:3], v150 offset:34816\n"
    "v_mfma_f32_16x16x32_bf16 v[28:31], a[20:23], a[60:63], v[28:31]\n"
    "ds_read_b128 a[32:35], v158 offset:12288\n"
    "ds_read_b128 a[36:39], v158 offset:14336\n"
    "v_mfma_f32_16x16x32_bf16 v[32:35], a[20:23], a[64:67], v[32:35]\n"
    "buffer_load_dwordx4 v145, s[44:47], s71 offen offset:1024 lds\n"
    "ds_read_b128 a[40:43], v158 offset:16384\n"
    "v_mfma_f32_16x16x32_bf16 v[36:39], a[20:23], a[68:71], v[36:39]\n"
    "ds_read_b128 a[44:47], v158 offset:18432\n"
    "ds_read_b128 a[48:51], v158 offset:20480\n"
    "v_mfma_f32_16x16x32_bf16 v[40:43], a[20:23], a[72:75], v[40:43]\n"
    "ds_read_b128 a[52:55], v158 offset:22528\n"
    "ds_read_b64_tr_b16 a[4:5], v151 offset:32768\n"
    "v_mfma_f32_16x16x32_bf16 v[44:47], a[20:23], a[76:79], v[44:47]\n"
    "ds_read_b64_tr_b16 a[6:7], v151 offset:34816\n"
    "ds_read_b64_tr_b16 a[8:9], v152 offset:32768\n"
    "v_mfma_f32_16x16x32_bf16 v[48:51], a[24:27], a[56:59], v[48:51]\n"
    "buffer_load_dwordx4 v146, s[44:47], s71 offen offset:2048 lds\n"
    "ds_read_b64_tr_b16 a[10:11], v152 offset:34816\n"
    "v_mfma_f32_16x16x32_bf16 v[52:55], a[24:27], a[60:63], v[52:55]\n"
    "ds_read_b64_tr_b16 a[12:13], v153 offset:32768\n"
    "ds_read_b64_tr_b16 a[14:15], v153 offset:34816\n"
    "v_mfma_f32_16x16x32_bf16 v[56:59], a[24:27], a[64:67], v[56:59]\n"
    "v_mfma_f32_16x16x32_bf16 v[60:63], a[24:27], a[68:71], v[60:63]\n"
    "v_mfma_f32_16x16x32_bf16 v[64:67], a[24:27], a[72:75], v[64:67]\n"
    "buffer_load_dwordx4 v147, s[44:47], s71 offen offset:3072 lds\n"
    "s_add_u32 s71, s71, 262144\n"
    "v_mfma_f32_16x16x32_bf16 v[68:71], a[24:27], a[76:79], v[68:71]\n"
    "s_cmp_ge_u32 s71, 0x380000\n"
    "s_cselect_b32 s71, s67, s71\n"
    "v_mfma_f32_16x16x32_bf16 v[72:75], a[28:31], a[56:59], v[72:75]\n"
    "s_add_u32 m0, s69, 81920\n"
    "v_mfma_f32_16x16x32_bf16 v[76:79], a[28:31], a[60:63], v[76:79]\n"
    "v_mfma_f32_16x16x32_bf16 v[80:83], a[28:31], a[64:67], v[80:83]\n"
    "buffer_load_dwordx4 v144, s[44:47], s71 offen offset:0 lds\n"
    "v_mfma_f32_16x16x32_bf16 v[84:87], a[28:31], a[68:71], v[84:87]\n"
    "v_mfma_f32_16x16x32_bf16 v[88:91], a[28:31], a[72:75], v[88:91]\n"
    "v_mfma_f32_16x16x32_bf16 v[92:95], a[28:31], a[76:79], v[92:95]\n"
    "s_waitcnt lgkmcnt(6)\n"
    "v_mfma_f32_16x16x32_bf16 v[0:3], a[0:3], a[32:35], v[0:3]\n"
    "buffer_load_dwordx4 v145, s[44:47], s71 offen offset:1024 lds\n"
    "v_mfma_f32_16x16x32_bf16 v[4:7], a[0:3], a[36:39], v[4:7]\n"
    "v_mfma_f32_16x16x32_bf16 v[8:11], a[0:3], a[40:43], v[8:11]\n"
    "v_mfma_f32_16x16x32_bf16 v[12:15], a[0:3], a[44:47], v[12:15]\n"
    "v_mfma_f32_16x16x32_bf16 v[16:19], a[0:3], a[48:51], v[16:19]\n"
    "buffer_load_dwordx4 v146, s[44:47], s71 offen offset:2048 lds\n"
    "v_mfma_f32_16x16x32_bf16 v[20:23], a[0:3], a[52:55], v[20:23]\n"
    "s_waitcnt vmcnt(10) lgkmcnt(0)\n"
    "s_barrier\n"
    "v_mfma_f32_16x16x32_bf16 v[24:27], a[4:7], a[32:35], v[24:27]\n"
    "ds_read_b64_tr_b16 a[16:17], v150 offset:49152\n"
    "ds_read_b64_tr_b16 a[18:19], v150 offset:51200\n"
    "v_mfma_f32_16x16x32_bf16 v[28:31], a[4:7], a[36:39], v[28:31]\n"
    "ds_read_b128 a[56:59], v159 offset:12288\n"
    "ds_read_b128 a[60:63], v159 offset:14336\n"
    "v_mfma_f32_16x16x32_bf16 v[32:35], a[4:7], a[40:43], v[32:35]\n"
    "buffer_load_dwordx4 v147, s[44:47], s71 offen offset:3072 lds\n"
    "ds_read_b128 a[64:67], v159 offset:16384\n"
    "v_mfma_f32_16x16x32_bf16 v[36:39], a[4:7], a[44:47], v[36:39]\n"
    "ds_read_b128 a[68:71], v159 offset:18432\n"
    "s_add_u32 s71, s71, 262144\n"
    "v_mfma_f32_16x16x32_bf16 v[40:43], a[4:7], a[48:51], v[40:43]\n"
    "s_cmp_ge_u32 s71, 0x380000\n"
    "s_cselect_b32 s71, s67, s71\n"
    "v_mfma_f32_16x16x32_bf16 v[44:47], a[4:7], a[52:55], v[44:47]\n"
    "s_add_u32 m0, s68, 151552\n"
    "ds_read_b128 a[72:75], v159 offset:20480\n"
    "v_mfma_f32_16x16x32_bf16 v[48:51], a[8:11], a[32:35], v[48:51]\n"
    "buffer_load_dwordx4 v179, s[40:43], s95 offen offset:0 lds\n"
    "ds_read_b128 a[76:79], v159 offset:22528\n"
    "v_mfma_f32_16x16x32_bf16 v[52:55], a[8:11], a[36:39], v[52:55]\n"
    "ds_read_b64_tr_b16 a[20:21], v151 offset:49152\n"
    "ds_read_b64_tr_b16 a[22:23], v151 offset:51200\n"
    "v_mfma_f32_16x16x32_bf16 v[56:59], a[8:11], a[40:43], v[56:59]\n"
    "ds_read_b64_tr_b16 a[24:25], v152 offset:49152\n"
    "ds_read_b64_tr_b16 a[26:27], v152 offset:51200\n"
    "v_mfma_f32_16x16x32_bf16 v[60:63], a[8:11], a[44:47], v[60:63]\n"
    "ds_read_b64_tr_b16 a[28:29], v153 offset:49152\n"
    "ds_read_b64_tr_b16 a[30:31], v153 offset:51200\n"
    "v_mfma_f32_16x16x32_bf16 v[64:67], a[8:11], a[48:51], v[64:67]\n"
    "buffer_load_dwordx4 v180, s[40:43], s95 offen offset:1024 lds\n"
    "v_mfma_f32_16x16x32_bf16 v[68:71], a[8:11], a[52:55], v[68:71]\n"
    "v_mfma_f32_16x16x32_bf16 v[72:75], a[12:15], a[32:35], v[72:75]\n"
    "v_mfma_f32_16x16x32_bf16 v[76:79], a[12:15], a[36:39], v[76:79]\n"
    "v_mfma_f32_16x16x32_bf16 v[80:83], a[12:15], a[40:43], v[80:83]\n"
    "buffer_load_dwordx4 v181, s[40:43], s95 offen offset:2048 lds\n"
    "s_add_u32 s95, s95, 128\n"
    "v_mfma_f32_16x16x32_bf16 v[84:87], a[12:15], a[44:47], v[84:87]\n"
    "s_cmp_eq_u32 s95, 0x380\n"
    "s_cselect_b32 s95, 0, s95\n"
    "v_mfma_f32_16x16x32_bf16 v[88:91], a[12:15], a[48:51], v[88:91]\n"
    "s_add_u32 m0, s69, 98304\n"
    "v_mfma_f32_16x16x32_bf16 v[92:95], a[12:15], a[52:55], v[92:95]\n"
    "s_waitcnt lgkmcnt(6)\n"
    "v_mfma_f32_16x16x32_bf16 v[0:3], a[16:19], a[56:59], v[0:3]\n"
    "buffer_load_dwordx4 v144, s[44:47], s71 offen offset:0 lds\n"
    "v_mfma_f32_16x16x32_bf16 v[4:7], a[16:19], a[60:63], v[4:7]\n"
    "v_mfma_f32_16x16x32_bf16 v[8:11], a[16:19], a[64:67], v[8:11]\n"
    "v_mfma_f32_16x16x32_bf16 v[12:15], a[16:19], a[68:71], v[12:15]\n"
    "v_mfma_f32_16x16x32_bf16 v[16:19], a[16:19], a[72:75], v[16:19]\n"
    "buffer_load_dwordx4 v145, s[44:47], s71 offen offset:1024 lds\n"
    "v_mfma_f32_16x16x32_bf16 v[20:23], a[16:19], a[76:79], v[20:23]\n"
    "s_waitcnt vmcnt(9) lgkmcnt(0)\n"
    "s_barrier\n"
    "v_mfma_f32_16x16x32_bf16 v[24:27], a[20:23], a[56:59], v[24:27]\n"
    "ds_read_b64_tr_b16 a[0:1], v154 offset:0\n"
    "ds_read_b64_tr_b16 a[2:3], v154 offset:2048\n"
    "v_mfma_f32_16x16x32_bf16 v[28:31], a[20:23], a[60:63], v[28:31]\n"
    "ds_read_b128 a[32:35], v158 offset:24576\n"
    "ds_read_b128 a[36:39], v158 offset:26624\n"
    "v_mfma_f32_16x16x32_bf16 v[32:35], a[20:23], a[64:67], v[32:35]\n"
    "buffer_load_dwordx4 v146, s[44:47], s71 offen offset:2048 lds\n"
    "ds_read_b128 a[40:43], v158 offset:28672\n"
    "v_mfma_f32_16x16x32_bf16 v[36:39], a[20:23], a[68:71], v[36:39]\n"
    "ds_read_b128 a[44:47], v158 offset:30720\n"
    "ds_read_b128 a[48:51], v158 offset:32768\n"
    "v_mfma_f32_16x16x32_bf16 v[40:43], a[20:23], a[72:75], v[40:43]\n"
    "ds_read_b128 a[52:55], v158 offset:34816\n"
    "ds_read_b64_tr_b16 a[4:5], v155 offset:0\n"
    "v_mfma_f32_16x16x32_bf16 v[44:47], a[20:23], a[76:79], v[44:47]\n"
    "ds_read_b64_tr_b16 a[6:7], v155 offset:2048\n"
    "ds_read_b64_tr_b16 a[8:9], v156 offset:0\n"
    "v_mfma_f32_16x16x32_bf16 v[48:51], a[24:27], a[56:59], v[48:51]\n"
    "buffer_load_dwordx4 v147, s[44:47], s71 offen offset:3072 lds\n"
    "s_add_u32 s71, s71, 262144\n"
    "v_mfma_f32_16x16x32_bf16 v[52:55], a[24:27], a[60:63], v[52:55]\n"
    "s_cmp_ge_u32 s71, 0x380000\n"
    "s_cselect_b32 s71, s67, s71\n"
    "v_mfma_f32_16x16x32_bf16 v[56:59], a[24:27], a[64:67], v[56:59]\n"
    "s_add_u32 m0, s69, 0\n"
    "ds_read_b64_tr_b16 a[10:11], v156 offset:2048\n"
    "v_mfma_f32_16x16x32_bf16 v[60:63], a[24:27], a[68:71], v[60:63]\n"
    "ds_read_b64_tr_b16 a[12:13], v157 offset:0\n"
    "ds_read_b64_tr_b16 a[14:15], v157 offset:2048\n"
    "v_mfma_f32_16x16x32_bf16 v[64:67], a[24:27], a[72:75], v[64:67]\n"
    "buffer_load_dwordx4 v144, s[44:47], s71 offen offset:0 lds\n"
    "v_mfma_f32_16x16x32_bf16 v[68:71], a[24:27], a[76:79], v[68:71]\n"
    "v_mfma_f32_16x16x32_bf16 v[72:75], a[28:31], a[56:59], v[72:75]\n"
    "v_mfma_f32_16x16x32_bf16 v[76:79], a[28:31], a[60:63], v[76:79]\n"
    "v_mfma_f32_16x16x32_bf16 v[80:83], a[28:31], a[64:67], v[80:83]\n"
    "buffer_load_dwordx4 v145, s[44:47], s71 offen offset:1024 lds\n"
    "v_mfma_f32_16x16x32_bf16 v[84:87], a[28:31], a[68:71], v[84:87]\n"
    "v_mfma_f32_16x16x32_bf16 v[88:91], a[28:31], a[72:75], v[88:91]\n"
    "v_mfma_f32_16x16x32_bf16 v[92:95], a[28:31], a[76:79], v[92:95]\n"
    "s_waitcnt lgkmcnt(6)\n"
    "v_mfma_f32_16x16x32_bf16 v[0:3], a[0:3], a[32:35], v[0:3]\n"
    "buffer_load_dwordx4 v146, s[44:47], s71 offen offset:2048 lds\n"
    "v_mfma_f32_16x16x32_bf16 v[4:7], a[0:3], a[36:39], v[4:7]\n"
    "v_mfma_f32_16x16x32_bf16 v[8:11], a[0:3], a[40:43], v[8:11]\n"
    "v_mfma_f32_16x16x32_bf16 v[12:15], a[0:3], a[44:47], v[12:15]\n"
    "v_mfma_f32_16x16x32_bf16 v[16:19], a[0:3], a[48:51], v[16:19]\n"
    "buffer_load_dwordx4 v147, s[44:47], s71 offen offset:3072 lds\n"
    "s_add_u32 s71, s71, 262144\n"
    "v_mfma_f32_16x16x32_bf16 v[20:23], a[0:3], a[52:55], v[20:23]\n"
    "s_waitcnt vmcnt(11) lgkmcnt(0)\n"
    "s_barrier\n"
    "v_mfma_f32_16x16x32_bf16 v[24:27], a[4:7], a[32:35], v[24:27]\n"
    "ds_read_b64_tr_b16 a[16:17], v154 offset:16384\n"
    "ds_read_b64_tr_b16 a[18:19], v154 offset:18432\n"
    "v_mfma_f32_16x16x32_bf16 v[28:31], a[4:7], a[36:39], v[28:31]\n"
    "s_cmp_ge_u32 s71, 0x380000\n"
    "s_cselect_b32 s71, s67, s71\n"
    "v_mfma_f32_16x16x32_bf16 v[32:35], a[4:7], a[40:43], v[32:35]\n"
    "s_add_u32 m0, s68, 114688\n"
    "ds_read_b128 a[56:59], v159 offset:24576\n"
    "v_mfma_f32_16x16x32_bf16 v[36:39], a[4:7], a[44:47], v[36:39]\n"
    "buffer_load_dwordx4 v179, s[40:43], s95 offen offset:0 lds\n"
    "ds_read_b128 a[60:63], v159 offset:26624\n"
    "v_mfma_f32_16x16x32_bf16 v[40:43], a[4:7], a[48:51], v[40:43]\n"
    "ds_read_b128 a[64:67], v159 offset:28672\n"
    "ds_read_b128 a[68:71], v159 offset:30720\n"
    "v_mfma_f32_16x16x32_bf16 v[44:47], a[4:7], a[52:55], v[44:47]\n"
    "ds_read_b128 a[72:75], v159 offset:32768\n"
    "ds_read_b128 a[76:79], v159 offset:34816\n"
    "v_mfma_f32_16x16x32_bf16 v[48:51], a[8:11], a[32:35], v[48:51]\n"
    "buffer_load_dwordx4 v180, s[40:43], s95 offen offset:1024 lds\n"
    "ds_read_b64_tr_b16 a[20:21], v155 offset:16384\n"
    "v_mfma_f32_16x16x32_bf16 v[52:55], a[8:11], a[36:39], v[52:55]\n"
    "ds_read_b64_tr_b16 a[22:23], v155 offset:18432\n"
    "ds_read_b64_tr_b16 a[24:25], v156 offset:16384\n"
    "v_mfma_f32_16x16x32_bf16 v[56:59], a[8:11], a[40:43], v[56:59]\n"
    "ds_read_b64_tr_b16 a[26:27], v156 offset:18432\n"
    "ds_read_b64_tr_b16 a[28:29], v157 offset:16384\n"
    "v_mfma_f32_16x16x32_bf16 v[60:63], a[8:11], a[44:47], v[60:63]\n"
    "ds_read_b64_tr_b16 a[30:31], v157 offset:18432\n"
    "v_mfma_f32_16x16x32_bf16 v[64:67], a[8:11], a[48:51], v[64:67]\n"
    "buffer_load_dwordx4 v181, s[40:43], s95 offen offset:2048 lds\n"
    "s_add_u32 s95, s95, 128\n"
    "v_mfma_f32_16x16x32_bf16 v[68:71], a[8:11], a[52:55], v[68:71]\n"
    "s_cmp_eq_u32 s95, 0x380\n"
    "s_cselect_b32 s95, 0, s95\n"
    "v_mfma_f32_16x16x32_bf16 v[72:75], a[12:15], a[32:35], v[72:75]\n"
    "s_add_u32 m0, s69, 16384\n"
    "v_mfma_f32_16x16x32_bf16 v[76:79], a[12:15], a[36:39], v[76:79]\n"
    "v_mfma_f32_16x16x32_bf16 v[80:83], a[12:15], a[40:43], v[80:83]\n"
    "buffer_load_dwordx4 v144, s[44:47], s71 offen offset:0 lds\n"
    "v_mfma_f32_16x16x32_bf16 v[84:87], a[12:15], a[44:47], v[84:87]\n"
    "v_mfma_f32_16x16x32_bf16 v[88:91], a[12:15], a[48:51], v[88:91]\n"
    "v_mfma_f32_16x16x32_bf16 v[92:95], a[12:15], a[52:55], v[92:95]\n"
    "s_waitcnt lgkmcnt(6)\n"
    "v_mfma_f32_16x16x32_bf16 v[0:3], a[16:19], a[56:59], v[0:3]\n"
    "buffer_load_dwordx4 v145, s[44:47], s71 offen offset:1024 lds\n"
    "v_mfma_f32_16x16x32_bf16 v[4:7], a[16:19], a[60:63], v[4:7]\n"
    "v_mfma_f32_16x16x32_bf16 v[8:11], a[16:19], a[64:67], v[8:11]\n"
    "v_mfma_f32_16x16x32_bf16 v[12:15], a[16:19], a[68:71], v[12:15]\n"
    "v_mfma_f32_16x16x32_bf16 v[16:19], a[16:19], a[72:75], v[16:19]\n"
    "buffer_load_dwordx4 v146, s[44:47], s71 offen offset:2048 lds\n"
    "v_mfma_f32_16x16x32_bf16 v[20:23], a[16:19], a[76:79], v[20:23]\n"
    "s_waitcnt vmcnt(10) lgkmcnt(0)\n"
    "s_barrier\n"
    "v_mfma_f32_16x16x32_bf16 v[24:27], a[20:23], a[56:59], v[24:27]\n"
    "ds_read_b64_tr_b16 a[0:1], v154 offset:32768\n"
    "ds_read_b64_tr_b16 a[2:3], v154 offset:34816\n"
    "v_mfma_f32_16x16x32_bf16 v[28:31], a[20:23], a[60:63], v[28:31]\n"
    "ds_read_b128 a[32:35], v158 offset:36864\n"
    "ds_read_b128 a[36:39], v158 offset:38912\n"
    "v_mfma_f32_16x16x32_bf16 v[32:35], a[20:23], a[64:67], v[32:35]\n"
    "buffer_load_dwordx4 v147, s[44:47], s71 offen offset:3072 lds\n"
    "ds_read_b128 a[40:43], v158 offset:40960\n"
    "v_mfma_f32_16x16x32_bf16 v[36:39], a[20:23], a[68:71], v[36:39]\n"
    "ds_read_b128 a[44:47], v158 offset:43008\n"
    "s_add_u32 s71, s71, 262144\n"
    "v_mfma_f32_16x16x32_bf16 v[40:43], a[20:23], a[72:75], v[40:43]\n"
    "s_cmp_ge_u32 s71, 0x380000\n"
    "s_cselect_b32 s71, s67, s71\n"
    "v_mfma_f32_16x16x32_bf16 v[44:47], a[20:23], a[76:79], v[44:47]\n"
    "s_add_u32 m0, s69, 32768\n"
    "ds_read_b128 a[48:51], v158 offset:45056\n"
    "v_mfma_f32_16x16x32_bf16 v[48:51], a[24:27], a[56:59], v[48:51]\n"
    "buffer_load_dwordx4 v144, s[44:47], s71 offen offset:0 lds\n"
    "ds_read_b128 a[52:55], v158 offset:47104\n"
    "v_mfma_f32_16x16x32_bf16 v[52:55], a[24:27], a[60:63], v[52:55]\n"
    "ds_read_b64_tr_b16 a[4:5], v155 offset:32768\n"
    "ds_read_b64_tr_b16 a[6:7], v155 offset:34816\n"
    "v_mfma_f32_16x16x32_bf16 v[56:59], a[24:27], a[64:67], v[56:59]\n"
    "ds_read_b64_tr_b16 a[8:9], v156 offset:32768\n"
    "ds_read_b64_tr_b16 a[10:11], v156 offset:34816\n"
    "v_mfma_f32_16x16x32_bf16 v[60:63], a[24:27], a[68:71], v[60:63]\n"
    "ds_read_b64_tr_b16 a[12:13], v157 offset:32768\n"
    "ds_read_b64_tr_b16 a[14:15], v157 offset:34816\n"
    "v_mfma_f32_16x16x32_bf16 v[64:67], a[24:27], a[72:75], v[64:67]\n"
    "buffer_load_dwordx4 v145, s[44:47], s71 offen offset:1024 lds\n"
    "v_mfma_f32_16x16x32_bf16 v[68:71], a[24:27], a[76:79], v[68:71]\n"
    "v_mfma_f32_16x16x32_bf16 v[72:75], a[28:31], a[56:59], v[72:75]\n"
    "v_mfma_f32_16x16x32_bf16 v[76:79], a[28:31], a[60:63], v[76:79]\n"
    "v_mfma_f32_16x16x32_bf16 v[80:83], a[28:31], a[64:67], v[80:83]\n"
    "buffer_load_dwordx4 v146, s[44:47], s71 offen offset:2048 lds\n"
    "v_mfma_f32_16x16x32_bf16 v[84:87], a[28:31], a[68:71], v[84:87]\n"
    "v_mfma_f32_16x16x32_bf16 v[88:91], a[28:31], a[72:75], v[88:91]\n"
    "v_mfma_f32_16x16x32_bf16 v[92:95], a[28:31], a[76:79], v[92:95]\n"
    "s_waitcnt lgkmcnt(6)\n"
    "v_mfma_f32_16x16x32_bf16 v[0:3], a[0:3], a[32:35], v[0:3]\n"
    "buffer_load_dwordx4 v147, s[44:47], s71 offen offset:3072 lds\n"
    "s_add_u32 s71, s71, 262144\n"
    "v_mfma_f32_16x16x32_bf16 v[4:7], a[0:3], a[36:39], v[4:7]\n"
    "s_cmp_ge_u32 s71, 0x380000\n"
    "s_cselect_b32 s71, s67, s71\n"
    "v_mfma_f32_16x16x32_bf16 v[8:11], a[0:3], a[40:43], v[8:11]\n"
    "s_add_u32 m0, s68, 126976\n"
    "v_mfma_f32_16x16x32_bf16 v[12:15], a[0:3], a[44:47], v[12:15]\n"
    "v_mfma_f32_16x16x32_bf16 v[16:19], a[0:3], a[48:51], v[16:19]\n"
    "buffer_load_dwordx4 v179, s[40:43], s95 offen offset:0 lds\n"
    "v_mfma_f32_16x16x32_bf16 v[20:23], a[0:3], a[52:55], v[20:23]\n"
    "s_waitcnt vmcnt(12) lgkmcnt(0)\n"
    "s_barrier\n"
    "v_mfma_f32_16x16x32_bf16 v[24:27], a[4:7], a[32:35], v[24:27]\n"
    "ds_read_b64_tr_b16 a[16:17], v150 offset:0\n"
    "ds_read_b64_tr_b16 a[18:19], v150 offset:2048\n"
    "v_mfma_f32_16x16x32_bf16 v[28:31], a[4:7], a[36:39], v[28:31]\n"
    "ds_read_b128 a[56:59], v159 offset:36864\n"
    "ds_read_b128 a[60:63], v159 offset:38912\n"
    "v_mfma_f32_16x16x32_bf16 v[32:35], a[4:7], a[40:43], v[32:35]\n"
    "buffer_load_dwordx4 v180, s[40:43], s95 offen offset:1024 lds\n"
    "ds_read_b128 a[64:67], v159 offset:40960\n"
    "v_mfma_f32_16x16x32_bf16 v[36:39], a[4:7], a[44:47], v[36:39]\n"
    "ds_read_b128 a[68:71], v159 offset:43008\n"
    "ds_read_b128 a[72:75], v159 offset:45056\n"
    "v_mfma_f32_16x16x32_bf16 v[40:43], a[4:7], a[48:51], v[40:43]\n"
    "ds_read_b128 a[76:79], v159 offset:47104\n"
    "ds_read_b64_tr_b16 a[20:21], v151 offset:0\n"
    "v_mfma_f32_16x16x32_bf16 v[44:47], a[4:7], a[52:55], v[44:47]\n"
    "ds_read_b64_tr_b16 a[22:23], v151 offset:2048\n"
    "ds_read_b64_tr_b16 a[24:25], v152 offset:0\n"
    "v_mfma_f32_16x16x32_bf16 v[48:51], a[8:11], a[32:35], v[48:51]\n"
    "buffer_load_dwordx4 v181, s[40:43], s95 offen offset:2048 lds\n"
    "s_add_u32 s95, s95, 128\n"
    "v_mfma_f32_16x16x32_bf16 v[52:55], a[8:11], a[36:39], v[52:55]\n"
    "s_cmp_eq_u32 s95, 0x380\n"
    "s_cselect_b32 s95, 0, s95\n"
    "v_mfma_f32_16x16x32_bf16 v[56:59], a[8:11], a[40:43], v[56:59]\n"
    "s_add_u32 m0, s69, 49152\n"
    "ds_read_b64_tr_b16 a[26:27], v152 offset:2048\n"
    "v_mfma_f32_16x16x32_bf16 v[60:63], a[8:11], a[44:47], v[60:63]\n"
    "ds_read_b64_tr_b16 a[28:29], v153 offset:0\n"
    "ds_read_b64_tr_b16 a[30:31], v153 offset:2048\n"
    "v_mfma_f32_16x16x32_bf16 v[64:67], a[8:11], a[48:51], v[64:67]\n"
    "buffer_load_dwordx4 v144, s[44:47], s71 offen offset:0 lds\n"
    "v_mfma_f32_16x16x32_bf16 v[68:71], a[8:11], a[52:55], v[68:71]\n"
    "v_mfma_f32_16x16x32_bf16 v[72:75], a[12:15], a[32:35], v[72:75]\n"
    "v_mfma_f32_16x16x32_bf16 v[76:79], a[12:15], a[36:39], v[76:79]\n"
    "v_mfma_f32_16x16x32_bf16 v[80:83], a[12:15], a[40:43], v[80:83]\n"
    "buffer_load_dwordx4 v145, s[44:47], s71 offen offset:1024 lds\n"
    "v_mfma_f32_16x16x32_bf16 v[84:87], a[12:15], a[44:47], v[84:87]\n"
    "v_mfma_f32_16x16x32_bf16 v[88:91], a[12:15], a[48:51], v[88:91]\n"
    "v_mfma_f32_16x16x32_bf16 v[92:95], a[12:15], a[52:55], v[92:95]\n"
    "s_waitcnt lgkmcnt(6)\n"
    "v_mfma_f32_16x16x32_bf16 v[0:3], a[16:19], a[56:59], v[0:3]\n"
    "buffer_load_dwordx4 v146, s[44:47], s71 offen offset:2048 lds\n"
    "v_mfma_f32_16x16x32_bf16 v[4:7], a[16:19], a[60:63], v[4:7]\n"
    "v_mfma_f32_16x16x32_bf16 v[8:11], a[16:19], a[64:67], v[8:11]\n"
    "v_mfma_f32_16x16x32_bf16 v[12:15], a[16:19], a[68:71], v[12:15]\n"
    "v_mfma_f32_16x16x32_bf16 v[16:19], a[16:19], a[72:75], v[16:19]\n"
    "buffer_load_dwordx4 v147, s[44:47], s71 offen offset:3072 lds\n"
    "s_add_u32 s71, s71, 262144\n"
    "v_mfma_f32_16x16x32_bf16 v[20:23], a[16:19], a[76:79], v[20:23]\n"
    "s_waitcnt vmcnt(11) lgkmcnt(0)\n"
    "s_barrier\n"
    "v_mfma_f32_16x16x32_bf16 v[24:27], a[20:23], a[56:59], v[24:27]\n"
    "ds_read_b64_tr_b16 a[0:1], v150 offset:16384\n"
    "ds_read_b64_tr_b16 a[2:3], v150 offset:18432\n"
    "v_mfma_f32_16x16x32_bf16 v[28:31], a[20:23], a[60:63], v[28:31]\n"
    "s_cmp_ge_u32 s71, 0x380000\n"
    "s_cselect_b32 s71, s67, s71\n"
    "v_mfma_f32_16x16x32_bf16 v[32:35], a[20:23], a[64:67], v[32:35]\n"
    "s_add_u32 m0, s69, 65536\n"
    "ds_read_b128 a[32:35], v158 offset:0\n"
    "v_mfma_f32_16x16x32_bf16 v[36:39], a[20:23], a[68:71], v[36:39]\n"
    "buffer_load_dwordx4 v144, s[44:47], s71 offen offset:0 lds\n"
    "ds_read_b128 a[36:39], v158 offset:2048\n"
    "v_mfma_f32_16x16x32_bf16 v[40:43], a[20:23], a[72:75], v[40:43]\n"
    "ds_read_b128 a[40:43], v158 offset:4096\n"
    "ds_read_b128 a[44:47], v158 offset:6144\n"
    "v_mfma_f32_16x16x32_bf16 v[44:47], a[20:23], a[76:79], v[44:47]\n"
    "ds_read_b128 a[48:51], v158 offset:8192\n"
    "ds_read_b128 a[52:55], v158 offset:10240\n"
    "v_mfma_f32_16x16x32_bf16 v[48:51], a[24:27], a[56:59], v[48:51]\n"
    "buffer_load_dwordx4 v145, s[44:47], s71 offen offset:1024 lds\n"
    "ds_read_b64_tr_b16 a[4:5], v151 offset:16384\n"
    "v_mfma_f32_16x16x32_bf16 v[52:55], a[24:27], a[60:63], v[52:55]\n"
    "ds_read_b64_tr_b16 a[6:7], v151 offset:18432\n"
    "ds_read_b64_tr_b16 a[8:9], v152 offset:16384\n"
    "v_mfma_f32_16x16x32_bf16 v[56:59], a[24:27], a[64:67], v[56:59]\n"
    "ds_read_b64_tr_b16 a[10:11], v152 offset:18432\n"
    "ds_read_b64_tr_b16 a[12:13], v153 offset:16384\n"
    "v_mfma_f32_16x16x32_bf16 v[60:63], a[24:27], a[68:71], v[60:63]\n"
    "ds_read_b64_tr_b16 a[14:15], v153 offset:18432\n"
    "v_mfma_f32_16x16x32_bf16 v[64:67], a[24:27], a[72:75], v[64:67]\n"
    "buffer_load_dwordx4 v146, s[44:47], s71 offen offset:2048 lds\n"
    "v_mfma_f32_16x16x32_bf16 v[68:71], a[24:27], a[76:79], v[68:71]\n"
    "v_mfma_f32_16x16x32_bf16 v[72:75], a[28:31], a[56:59], v[72:75]\n"
    "v_mfma_f32_16x16x32_bf16 v[76:79], a[28:31], a[60:63], v[76:79]\n"
    "v_mfma_f32_16x16x32_bf16 v[80:83], a[28:31], a[64:67], v[80:83]\n"
    "buffer_load_dwordx4 v147, s[44:47], s71 offen offset:3072 lds\n"
    "s_add_u32 s71, s71, 262144\n"
    "v_mfma_f32_16x16x32_bf16 v[84:87], a[28:31], a[68:71], v[84:87]\n"
    "s_cmp_ge_u32 s71, 0x380000\n"
    "s_cselect_b32 s71, s67, s71\n"
    "v_mfma_f32_16x16x32_bf16 v[88:91], a[28:31], a[72:75], v[88:91]\n"
    "s_add_u32 m0, s68, 139264\n"
    "v_mfma_f32_16x16x32_bf16 v[92:95], a[28:31], a[76:79], v[92:95]\n"
    "s_waitcnt lgkmcnt(6)\n"
    "v_mfma_f32_16x16x32_bf16 v[0:3], a[0:3], a[32:35], v[0:3]\n"
    "buffer_load_dwordx4 v179, s[40:43], s95 offen offset:0 lds\n"
    "v_mfma_f32_16x16x32_bf16 v[4:7], a[0:3], a[36:39], v[4:7]\n"
    "v_mfma_f32_16x16x32_bf16 v[8:11], a[0:3], a[40:43], v[8:11]\n"
    "v_mfma_f32_16x16x32_bf16 v[12:15], a[0:3], a[44:47], v[12:15]\n"
    "v_mfma_f32_16x16x32_bf16 v[16:19], a[0:3], a[48:51], v[16:19]\n"
    "buffer_load_dwordx4 v180, s[40:43], s95 offen offset:1024 lds\n"
    "v_mfma_f32_16x16x32_bf16 v[20:23], a[0:3], a[52:55], v[20:23]\n"
    "s_waitcnt vmcnt(13) lgkmcnt(0)\n"
    "s_barrier\n"
    "v_mfma_f32_16x16x32_bf16 v[24:27], a[4:7], a[32:35], v[24:27]\n"
    "ds_read_b64_tr_b16 a[16:17], v150 offset:32768\n"
    "ds_read_b64_tr_b16 a[18:19], v150 offset:34816\n"
    "v_mfma_f32_16x16x32_bf16 v[28:31], a[4:7], a[36:39], v[28:31]\n"
    "ds_read_b128 a[56:59], v159 offset:0\n"
    "ds_read_b128 a[60:63], v159 offset:2048\n"
    "v_mfma_f32_16x16x32_bf16 v[32:35], a[4:7], a[40:43], v[32:35]\n"
    "buffer_load_dwordx4 v181, s[40:43], s95 offen offset:2048 lds\n"
    "ds_read_b128 a[64:67], v159 offset:4096\n"
    "v_mfma_f32_16x16x32_bf16 v[36:39], a[4:7], a[44:47], v[36:39]\n"
    "ds_read_b128 a[68:71], v159 offset:6144\n"
    "s_add_u32 s95, s95, 128\n"
    "v_mfma_f32_16x16x32_bf16 v[40:43], a[4:7], a[48:51], v[40:43]\n"
    "s_cmp_eq_u32 s95, 0x380\n"
    "s_cselect_b32 s95, 0, s95\n"
    "v_mfma_f32_16x16x32_bf16 v[44:47], a[4:7], a[52:55], v[44:47]\n"
    "s_add_u32 m0, s69, 81920\n"
    "ds_read_b128 a[72:75], v159 offset:8192\n"
    "v_mfma_f32_16x16x32_bf16 v[48:51], a[8:11], a[32:35], v[48:51]\n"
    "buffer_load_dwordx4 v144, s[44:47], s71 offen offset:0 lds\n"
    "ds_read_b128 a[76:79], v159 offset:10240\n"
    "v_mfma_f32_16x16x32_bf16 v[52:55], a[8:11], a[36:39], v[52:55]\n"
    "ds_read_b64_tr_b16 a[20:21], v151 offset:32768\n"
    "ds_read_b64_tr_b16 a[22:23], v151 offset:34816\n"
    "v_mfma_f32_16x16x32_bf16 v[56:59], a[8:11], a[40:43], v[56:59]\n"
    "ds_read_b64_tr_b16 a[24:25], v152 offset:32768\n"
    "ds_read_b64_tr_b16 a[26:27], v152 offset:34816\n"
    "v_mfma_f32_16x16x32_bf16 v[60:63], a[8:11], a[44:47], v[60:63]\n"
    "ds_read_b64_tr_b16 a[28:29], v153 offset:32768\n"
    "ds_read_b64_tr_b16 a[30:31], v153 offset:34816\n"
    "v_mfma_f32_16x16x32_bf16 v[64:67], a[8:11], a[48:51], v[64:67]\n"
    "buffer_load_dwordx4 v145, s[44:47], s71 offen offset:1024 lds\n"
    "v_mfma_f32_16x16x32_bf16 v[68:71], a[8:11], a[52:55], v[68:71]\n"
    "v_mfma_f32_16x16x32_bf16 v[72:75], a[12:15], a[32:35], v[72:75]\n"
    "v_mfma_f32_16x16x32_bf16 v[76:79], a[12:15], a[36:39], v[76:79]\n"
    "v_mfma_f32_16x16x32_bf16 v[80:83], a[12:15], a[40:43], v[80:83]\n"
    "buffer_load_dwordx4 v146, s[44:47], s71 offen offset:2048 lds\n"
    "v_mfma_f32_16x16x32_bf16 v[84:87], a[12:15], a[44:47], v[84:87]\n"
    "v_mfma_f32_16x16x32_bf16 v[88:91], a[12:15], a[48:51], v[88:91]\n"
    "v_mfma_f32_16x16x32_bf16 v[92:95], a[12:15], a[52:55], v[92:95]\n"
    "s_waitcnt lgkmcnt(6)\n"
    "v_mfma_f32_16x16x32_bf16 v[0:3], a[16:19], a[56:59], v[0:3]\n"
    "buffer_load_dwordx4 v147, s[44:47], s71 offen offset:3072 lds\n"
    "s_add_u32 s71, s71, 262144\n"
    "v_mfma_f32_16x16x32_bf16 v[4:7], a[16:19], a[60:63], v[4:7]\n"
    "s_cmp_ge_u32 s71, 0x380000\n"
    "s_cselect_b32 s71, s67, s71\n"
    "v_mfma_f32_16x16x32_bf16 v[8:11], a[16:19], a[64:67], v[8:11]\n"
    "s_add_u32 m0, s69, 98304\n"
    "v_mfma_f32_16x16x32_bf16 v[12:15], a[16:19], a[68:71], v[12:15]\n"
    "v_mfma_f32_16x16x32_bf16 v[16:19], a[16:19], a[72:75], v[16:19]\n"
    "buffer_load_dwordx4 v144, s[44:47], s71 offen offset:0 lds\n"
    "v_mfma_f32_16x16x32_bf16 v[20:23], a[16:19], a[76:79], v[20:23]\n"
    "s_waitcnt vmcnt(12) lgkmcnt(0)\n"
    "s_barrier\n"
    "v_mfma_f32_16x16x32_bf16 v[24:27], a[20:23], a[56:59], v[24:27]\n"
    "ds_read_b64_tr_b16 a[0:1], v150 offset:49152\n"
    "ds_read_b64_tr_b16 a[2:3], v150 offset:51200\n"
    "v_mfma_f32_16x16x32_bf16 v[28:31], a[20:23], a[60:63], v[28:31]\n"
    "ds_read_b128 a[32:35], v158 offset:12288\n"
    "ds_read_b128 a[36:39], v158 offset:14336\n"
    "v_mfma_f32_16x16x32_bf16 v[32:35], a[20:23], a[64:67], v[32:35]\n"
    "buffer_load_dwordx4 v145, s[44:47], s71 offen offset:1024 lds\n"
    "ds_read_b128 a[40:43], v158 offset:16384\n"
    "v_mfma_f32_16x16x32_bf16 v[36:39], a[20:23], a[68:71], v[36:39]\n"
    "ds_read_b128 a[44:47], v158 offset:18432\n"
    "ds_read_b128 a[48:51], v158 offset:20480\n"
    "v_mfma_f32_16x16x32_bf16 v[40:43], a[20:23], a[72:75], v[40:43]\n"
    "ds_read_b128 a[52:55], v158 offset:22528\n"
    "ds_read_b64_tr_b16 a[4:5], v151 offset:49152\n"
    "v_mfma_f32_16x16x32_bf16 v[44:47], a[20:23], a[76:79], v[44:47]\n"
    "ds_read_b64_tr_b16 a[6:7], v151 offset:51200\n"
    "ds_read_b64_tr_b16 a[8:9], v152 offset:49152\n"
    "v_mfma_f32_16x16x32_bf16 v[48:51], a[24:27], a[56:59], v[48:51]\n"
    "buffer_load_dwordx4 v146, s[44:47], s71 offen offset:2048 lds\n"
    "ds_read_b64_tr_b16 a[10:11], v152 offset:51200\n"
    "v_mfma_f32_16x16x32_bf16 v[52:55], a[24:27], a[60:63], v[52:55]\n"
    "ds_read_b64_tr_b16 a[12:13], v153 offset:49152\n"
    "ds_read_b64_tr_b16 a[14:15], v153 offset:51200\n"
    "v_mfma_f32_16x16x32_bf16 v[56:59], a[24:27], a[64:67], v[56:59]\n"
    "v_mfma_f32_16x16x32_bf16 v[60:63], a[24:27], a[68:71], v[60:63]\n"
    "v_mfma_f32_16x16x32_bf16 v[64:67], a[24:27], a[72:75], v[64:67]\n"
    "buffer_load_dwordx4 v147, s[44:47], s71 offen offset:3072 lds\n"
    "s_add_u32 s71, s71, 262144\n"
    "v_mfma_f32_16x16x32_bf16 v[68:71], a[24:27], a[76:79], v[68:71]\n"
    "s_cmp_ge_u32 s71, 0x380000\n"
    "s_cselect_b32 s71, s67, s71\n"
    "v_mfma_f32_16x16x32_bf16 v[72:75], a[28:31], a[56:59], v[72:75]\n"
    "s_add_u32 m0, s91, 0\n"
    "v_mfma_f32_16x16x32_bf16 v[76:79], a[28:31], a[60:63], v[76:79]\n"
    "v_mfma_f32_16x16x32_bf16 v[80:83], a[28:31], a[64:67], v[80:83]\n"
    "buffer_load_dwordx4 v200, s[48:51], s66 offen offset:0 nt lds\n"
    "v_mfma_f32_16x16x32_bf16 v[84:87], a[28:31], a[68:71], v[84:87]\n"
    "v_mfma_f32_16x16x32_bf16 v[88:91], a[28:31], a[72:75], v[88:91]\n"
    "v_mfma_f32_16x16x32_bf16 v[92:95], a[28:31], a[76:79], v[92:95]\n"
    "s_waitcnt lgkmcnt(6)\n"
    "v_mfma_f32_16x16x32_bf16 v[0:3], a[0:3], a[32:35], v[0:3]\n"
    "buffer_load_dwordx4 v201, s[48:51], s66 offen offset:1024 nt lds\n"
    "s_add_u32 m0, s91, 8192\n"
    "v_mfma_f32_16x16x32_bf16 v[4:7], a[0:3], a[36:39], v[4:7]\n"
    "v_mfma_f32_16x16x32_bf16 v[8:11], a[0:3], a[40:43], v[8:11]\n"
    "v_mfma_f32_16x16x32_bf16 v[12:15], a[0:3], a[44:47], v[12:15]\n"
    "v_mfma_f32_16x16x32_bf16 v[16:19], a[0:3], a[48:51], v[16:19]\n"
    "buffer_load_dwordx4 v202, s[48:51], s66 offen offset:0 nt lds\n"
    "v_mfma_f32_16x16x32_bf16 v[20:23], a[0:3], a[52:55], v[20:23]\n"
    "s_waitcnt vmcnt(14) lgkmcnt(0)\n"
    "s_barrier\n"
    "v_mfma_f32_16x16x32_bf16 v[24:27], a[4:7], a[32:35], v[24:27]\n"
    "ds_read_b64_tr_b16 a[16:17], v154 offset:0\n"
    "ds_read_b64_tr_b16 a[18:19], v154 offset:2048\n"
    "v_mfma_f32_16x16x32_bf16 v[28:31], a[4:7], a[36:39], v[28:31]\n"
    "ds_read_b128 a[56:59], v159 offset:12288\n"
    "ds_read_b128 a[60:63], v159 offset:14336\n"
    "v_mfma_f32_16x16x32_bf16 v[32:35], a[4:7], a[40:43], v[32:35]\n"
    "buffer_load_dwordx4 v203, s[48:51], s66 offen offset:1024 nt lds\n"
    "ds_read_b128 a[64:67], v159 offset:16384\n"
    "v_mfma_f32_16x16x32_bf16 v[36:39], a[4:7], a[44:47], v[36:39]\n"
    "ds_read_b128 a[68:71], v159 offset:18432\n"
    "s_add_u32 m0, s91, 16384\n"
    "v_mfma_f32_16x16x32_bf16 v[40:43], a[4:7], a[48:51], v[40:43]\n"
    "ds_read_b128 a[72:75], v159 offset:20480\n"
    "ds_read_b128 a[76:79], v159 offset:22528\n"
    "v_mfma_f32_16x16x32_bf16 v[44:47], a[4:7], a[52:55], v[44:47]\n"
    "ds_read_b64_tr_b16 a[20:21], v155 offset:0\n"
    "ds_read_b64_tr_b16 a[22:23], v155 offset:2048\n"
    "v_mfma_f32_16x16x32_bf16 v[48:51], a[8:11], a[32:35], v[48:51]\n"
    "buffer_load_dwordx4 v204, s[48:51], s66 offen offset:0 nt lds\n"
    "ds_read_b64_tr_b16 a[24:25], v156 offset:0\n"
    "v_mfma_f32_16x16x32_bf16 v[52:55], a[8:11], a[36:39], v[52:55]\n"
    "ds_read_b64_tr_b16 a[26:27], v156 offset:2048\n"
    "ds_read_b64_tr_b16 a[28:29], v157 offset:0\n"
    "v_mfma_f32_16x16x32_bf16 v[56:59], a[8:11], a[40:43], v[56:59]\n"
    "ds_read_b64_tr_b16 a[30:31], v157 offset:2048\n"
    "v_mfma_f32_16x16x32_bf16 v[60:63], a[8:11], a[44:47], v[60:63]\n"
    "v_mfma_f32_16x16x32_bf16 v[64:67], a[8:11], a[48:51], v[64:67]\n"
    "buffer_load_dwordx4 v205, s[48:51], s66 offen offset:1024 nt lds\n"
    "s_add_u32 m0, s91, 24576\n"
    "v_mfma_f32_16x16x32_bf16 v[68:71], a[8:11], a[52:55], v[68:71]\n"
    "v_mfma_f32_16x16x32_bf16 v[72:75], a[12:15], a[32:35], v[72:75]\n"
    "v_mfma_f32_16x16x32_bf16 v[76:79], a[12:15], a[36:39], v[76:79]\n"
    "v_mfma_f32_16x16x32_bf16 v[80:83], a[12:15], a[40:43], v[80:83]\n"
    "buffer_load_dwordx4 v206, s[48:51], s66 offen offset:0 nt lds\n"
    "v_mfma_f32_16x16x32_bf16 v[84:87], a[12:15], a[44:47], v[84:87]\n"
    "v_mfma_f32_16x16x32_bf16 v[88:91], a[12:15], a[48:51], v[88:91]\n"
    "v_mfma_f32_16x16x32_bf16 v[92:95], a[12:15], a[52:55], v[92:95]\n"
    "s_waitcnt lgkmcnt(6)\n"
    "v_mfma_f32_16x16x32_bf16 v[0:3], a[16:19], a[56:59], v[0:3]\n"
    "buffer_load_dwordx4 v207, s[48:51], s66 offen offset:1024 nt lds\n"
    "s_add_u32 s66, s66, 32768\n"
    "v_mfma_f32_16x16x32_bf16 v[4:7], a[16:19], a[60:63], v[4:7]\n"
    "s_add_u32 m0, s91, 32768\n"
    "v_mfma_f32_16x16x32_bf16 v[8:11], a[16:19], a[64:67], v[8:11]\n"
    "v_mfma_f32_16x16x32_bf16 v[12:15], a[16:19], a[68:71], v[12:15]\n"
    "v_mfma_f32_16x16x32_bf16 v[16:19], a[16:19], a[72:75], v[16:19]\n"
    "buffer_load_dwordx4 v200, s[48:51], s66 offen offset:0 nt lds\n"
    "v_mfma_f32_16x16x32_bf16 v[20:23], a[16:19], a[76:79], v[20:23]\n"
    "s_waitcnt vmcnt(9) lgkmcnt(0)\n"
    "s_barrier\n"
    "v_mfma_f32_16x16x32_bf16 v[24:27], a[20:23], a[56:59], v[24:27]\n"
    "ds_read_b64_tr_b16 a[0:1], v154 offset:16384\n"
    "ds_read_b64_tr_b16 a[2:3], v154 offset:18432\n"
    "v_mfma_f32_16x16x32_bf16 v[28:31], a[20:23], a[60:63], v[28:31]\n"
    "ds_read_b128 a[32:35], v158 offset:24576\n"
    "ds_read_b128 a[36:39], v158 offset:26624\n"
    "v_mfma_f32_16x16x32_bf16 v[32:35], a[20:23], a[64:67], v[32:35]\n"
    "buffer_load_dwordx4 v201, s[48:51], s66 offen offset:1024 nt lds\n"
    "ds_read_b128 a[40:43], v158 offset:28672\n"
    "v_mfma_f32_16x16x32_bf16 v[36:39], a[20:23], a[68:71], v[36:39]\n"
    "ds_read_b128 a[44:47], v158 offset:30720\n"
    "s_add_u32 m0, s91, 40960\n"
    "v_mfma_f32_16x16x32_bf16 v[40:43], a[20:23], a[72:75], v[40:43]\n"
    "ds_read_b128 a[48:51], v158 offset:32768\n"
    "ds_read_b128 a[52:55], v158 offset:34816\n"
    "v_mfma_f32_16x16x32_bf16 v[44:47], a[20:23], a[76:79], v[44:47]\n"
    "ds_read_b64_tr_b16 a[4:5], v155 offset:16384\n"
    "ds_read_b64_tr_b16 a[6:7], v155 offset:18432\n"
    "v_mfma_f32_16x16x32_bf16 v[48:51], a[24:27], a[56:59], v[48:51]\n"
    "buffer_load_dwordx4 v202, s[48:51], s66 offen offset:0 nt lds\n"
    "ds_read_b64_tr_b16 a[8:9], v156 offset:16384\n"
    "v_mfma_f32_16x16x32_bf16 v[52:55], a[24:27], a[60:63], v[52:55]\n"
    "ds_read_b64_tr_b16 a[10:11], v156 offset:18432\n"
    "ds_read_b64_tr_b16 a[12:13], v157 offset:16384\n"
    "v_mfma_f32_16x16x32_bf16 v[56:59], a[24:27], a[64:67], v[56:59]\n"
    "ds_read_b64_tr_b16 a[14:15], v157 offset:18432\n"
    "v_mfma_f32_16x16x32_bf16 v[60:63], a[24:27], a[68:71], v[60:63]\n"
    "v_mfma_f32_16x16x32_bf16 v[64:67], a[24:27], a[72:75], v[64:67]\n"
    "buffer_load_dwordx4 v203, s[48:51], s66 offen offset:1024 nt lds\n"
    "s_add_u32 m0, s91, 49152\n"
    "v_mfma_f32_16x16x32_bf16 v[68:71], a[24:27], a[76:79], v[68:71]\n"
    "v_mfma_f32_16x16x32_bf16 v[72:75], a[28:31], a[56:59], v[72:75]\n"
    "v_mfma_f32_16x16x32_bf16 v[76:79], a[28:31], a[60:63], v[76:79]\n"
    "v_mfma_f32_16x16x32_bf16 v[80:83], a[28:31], a[64:67], v[80:83]\n"
    "v_mfma_f32_16x16x32_bf16 v[84:87], a[28:31], a[68:71], v[84:87]\n"
    "v_mfma_f32_16x16x32_bf16 v[88:91], a[28:31], a[72:75], v[88:91]\n"
    "v_mfma_f32_16x16x32_bf16 v[92:95], a[28:31], a[76:79], v[92:95]\n"
    "s_waitcnt lgkmcnt(6)\n"
    "v_mfma_f32_16x16x32_bf16 v[0:3], a[0:3], a[32:35], v[0:3]\n"
    "ds_read_b64_tr_b16 a[16:17], v154 offset:32768\n"
    "ds_read_b64_tr_b16 a[18:19], v154 offset:34816\n"
    "v_mfma_f32_16x16x32_bf16 v[4:7], a[0:3], a[36:39], v[4:7]\n"
    "ds_read_b128 a[56:59], v159 offset:24576\n"
    "ds_read_b128 a[60:63], v159 offset:26624\n"
    "buffer_load_dwordx4 v204, s[48:51], s66 offen offset:0 nt lds\n"
    "v_mfma_f32_16x16x32_bf16 v[8:11], a[0:3], a[40:43], v[8:11]\n"
    "ds_read_b128 a[64:67], v159 offset:28672\n"
    "ds_read_b128 a[68:71], v159 offset:30720\n"
    "v_mfma_f32_16x16x32_bf16 v[12:15], a[0:3], a[44:47], v[12:15]\n"
    "ds_read_b128 a[72:75], v159 offset:32768\n"
    "ds_read_b128 a[76:79], v159 offset:34816\n"
    "v_mfma_f32_16x16x32_bf16 v[16:19], a[0:3], a[48:51], v[16:19]\n"
    "ds_read_b64_tr_b16 a[20:21], v155 offset:32768\n"
    "ds_read_b64_tr_b16 a[22:23], v155 offset:34816\n"
    "v_mfma_f32_16x16x32_bf16 v[20:23], a[0:3], a[52:55], v[20:23]\n"
    "ds_read_b64_tr_b16 a[24:25], v156 offset:32768\n"
    "ds_read_b64_tr_b16 a[26:27], v156 offset:34816\n"
    "s_waitcnt lgkmcnt(4)\n"
    "v_mfma_f32_16x16x32_bf16 v[0:3], a[16:19], a[56:59], v[0:3]\n"
    "ds_read_b64_tr_b16 a[28:29], v157 offset:32768\n"
    "ds_read_b64_tr_b16 a[30:31], v157 offset:34816\n"
    "v_mfma_f32_16x16x32_bf16 v[4:7], a[16:19], a[60:63], v[4:7]\n"
    "v_mfma_f32_16x16x32_bf16 v[8:11], a[16:19], a[64:67], v[8:11]\n"
    "buffer_load_dwordx4 v205, s[48:51], s66 offen offset:1024 nt lds\n"
    "s_add_u32 m0, s91, 57344\n"
    "v_mfma_f32_16x16x32_bf16 v[12:15], a[16:19], a[68:71], v[12:15]\n"
    "v_mfma_f32_16x16x32_bf16 v[16:19], a[16:19], a[72:75], v[16:19]\n"
    "v_mfma_f32_16x16x32_bf16 v[20:23], a[16:19], a[76:79], v[20:23]\n"
    "s_waitcnt lgkmcnt(0)\n"
    "s_barrier\n"
    "v_mfma_f32_16x16x32_bf16 v[24:27], a[4:7], a[32:35], v[24:27]\n"
    "v_mul_f32 v0, s72, v0\n"
    "v_mul_f32 v1, s72, v1\n"
    "v_mfma_f32_16x16x32_bf16 v[28:31], a[4:7], a[36:39], v[28:31]\n"
    "v_mul_f32 v2, s72, v2\n"
    "v_mul_f32 v3, s72, v3\n"
    "v_mfma_f32_16x16x32_bf16 v[32:35], a[4:7], a[40:43], v[32:35]\n"
    "v_cvt_pk_bf16_f32 v0, v0, v1\n"
    "v_cvt_pk_bf16_f32 v1, v2, v3\n"
    "v_mfma_f32_16x16x32_bf16 v[36:39], a[4:7], a[44:47], v[36:39]\n"
    "ds_write_b64 v162, v[0:1] offset:0\n"
    "v_mul_f32 v4, s72, v4\n"
    "v_mfma_f32_16x16x32_bf16 v[40:43], a[4:7], a[48:51], v[40:43]\n"
    "buffer_load_dwordx4 v206, s[48:51], s66 offen offset:0 nt lds\n"
    "v_mul_f32 v5, s72, v5\n"
    "v_mfma_f32_16x16x32_bf16 v[44:47], a[4:7], a[52:55], v[44:47]\n"
    "v_mul_f32 v6, s72, v6\n"
    "v_mul_f32 v7, s72, v7\n"
    "v_mfma_f32_16x16x32_bf16 v[24:27], a[20:23], a[56:59], v[24:27]\n"
    "v_cvt_pk_bf16_f32 v4, v4, v5\n"
    "v_cvt_pk_bf16_f32 v5, v6, v7\n"
    "v_mfma_f32_16x16x32_bf16 v[28:31], a[20:23], a[60:63], v[28:31]\n"
    "ds_write_b64 v162, v[4:5] offset:8192\n"
    "v_mul_f32 v8, s72, v8\n"
    "v_mfma_f32_16x16x32_bf16 v[32:35], a[20:23], a[64:67], v[32:35]\n"
    "v_mul_f32 v9, s72, v9\n"
    "v_mul_f32 v10, s72, v10\n"
    "v_mfma_f32_16x16x32_bf16 v[36:39], a[20:23], a[68:71], v[36:39]\n"
    "v_mul_f32 v11, s72, v11\n"
    "v_cvt_pk_bf16_f32 v8, v8, v9\n"
    "v_mfma_f32_16x16x32_bf16 v[40:43], a[20:23], a[72:75], v[40:43]\n"
    "v_cvt_pk_bf16_f32 v9, v10, v11\n"
    "ds_write_b64 v162, v[8:9] offset:16384\n"
    "v_mfma_f32_16x16x32_bf16 v[44:47], a[20:23], a[76:79], v[44:47]\n"
    "v_mul_f32 v12, s72, v12\n"
    "v_mul_f32 v13, s72, v13\n"
    "v_mfma_f32_16x16x32_bf16 v[48:51], a[8:11], a[32:35], v[48:51]\n"
    "buffer_load_dwordx4 v207, s[48:51], s66 offen offset:1024 nt lds\n"
    "s_add_u32 s66, s66, 32768\n"
    "v_mfma_f32_16x16x32_bf16 v[52:55], a[8:11], a[36:39], v[52:55]\n"
    "s_add_u32 s68, s91, 16384\n"
    "s_mov_b32 s67, s91\n"
    "v_mfma_f32_16x16x32_bf16 v[56:59], a[8:11], a[40:43], v[56:59]\n"
    "s_add_u32 m0, s91, 65536\n"
    "v_mul_f32 v14, s72, v14\n"
    "v_mfma_f32_16x16x32_bf16 v[60:63], a[8:11], a[44:47], v[60:63]\n"
    "v_mul_f32 v15, s72, v15\n"
    "v_cvt_pk_bf16_f32 v12, v12, v13\n"
    "v_mfma_f32_16x16x32_bf16 v[64:67], a[8:11], a[48:51], v[64:67]\n"
    "v_cvt_pk_bf16_f32 v13, v14, v15\n"
    "ds_write_b64 v162, v[12:13] offset:24576\n"
    "v_mfma_f32_16x16x32_bf16 v[68:71], a[8:11], a[52:55], v[68:71]\n"
    "v_mul_f32 v16, s72, v16\n"
    "v_mul_f32 v17, s72, v17\n"
    "v_mfma_f32_16x16x32_bf16 v[48:51], a[24:27], a[56:59], v[48:51]\n"
    "v_mul_f32 v18, s72, v18\n"
    "v_mul_f32 v19, s72, v19\n"
    "v_mfma_f32_16x16x32_bf16 v[52:55], a[24:27], a[60:63], v[52:55]\n"
    "v_cvt_pk_bf16_f32 v16, v16, v17\n"
    "v_cvt_pk_bf16_f32 v17, v18, v19\n"
    "v_mfma_f32_16x16x32_bf16 v[56:59], a[24:27], a[64:67], v[56:59]\n"
    "buffer_load_dwordx4 v208, s[52:55], s67 offen offset:0 nt lds\n"
    "ds_write_b64 v162, v[16:17] offset:32768\n"
    "v_mfma_f32_16x16x32_bf16 v[60:63], a[24:27], a[68:71], v[60:63]\n"
    "v_mul_f32 v20, s72, v20\n"
    "v_mul_f32 v21, s72, v21\n"
    "v_mfma_f32_16x16x32_bf16 v[64:67], a[24:27], a[72:75], v[64:67]\n"
    "v_mul_f32 v22, s72, v22\n"
    "v_mul_f32 v23, s72, v23\n"
    "v_mfma_f32_16x16x32_bf16 v[68:71], a[24:27], a[76:79], v[68:71]\n"
    "v_cvt_pk_bf16_f32 v20, v20, v21\n"
    "v_cvt_pk_bf16_f32 v21, v22, v23\n"
    "v_mfma_f32_16x16x32_bf16 v[72:75], a[12:15], a[32:35], v[72:75]\n"
    "ds_write_b64 v162, v[20:21] offset:40960\n"
    "v_mul_f32 v24, s72, v24\n"
    "v_mfma_f32_16x16x32_bf16 v[76:79], a[12:15], a[36:39], v[76:79]\n"
    "v_mul_f32 v25, s72, v25\n"
    "v_mul_f32 v26, s72, v26\n"
    "v_mfma_f32_16x16x32_bf16 v[80:83], a[12:15], a[40:43], v[80:83]\n"
    "v_mul_f32 v27, s72, v27\n"
    "v_cvt_pk_bf16_f32 v24, v24, v25\n"
    "v_mfma_f32_16x16x32_bf16 v[84:87], a[12:15], a[44:47], v[84:87]\n"
    "v_cvt_pk_bf16_f32 v25, v26, v27\n"
    "ds_write_b64 v163, v[24:25] offset:0\n"
    "v_mfma_f32_16x16x32_bf16 v[88:91], a[12:15], a[48:51], v[88:91]\n"
    "buffer_load_dwordx4 v209, s[52:55], s67 offen offset:1024 nt lds\n"
    "s_add_u32 m0, s91, 73728\n"
    "v_mfma_f32_16x16x32_bf16 v[92:95], a[12:15], a[52:55], v[92:95]\n"
    "v_mul_f32 v28, s72, v28\n"
    "v_mul_f32 v29, s72, v29\n"
    "v_mfma_f32_16x16x32_bf16 v[72:75], a[28:31], a[56:59], v[72:75]\n"
    "v_mul_f32 v30, s72, v30\n"
    "v_mul_f32 v31, s72, v31\n"
    "v_mfma_f32_16x16x32_bf16 v[76:79], a[28:31], a[60:63], v[76:79]\n"
    "v_cvt_pk_bf16_f32 v28, v28, v29\n"
    "v_cvt_pk_bf16_f32 v29, v30, v31\n"
    "v_mfma_f32_16x16x32_bf16 v[80:83], a[28:31], a[64:67], v[80:83]\n"
    "ds_write_b64 v163, v[28:29] offset:8192\n"
    "v_mul_f32 v32, s72, v32\n"
    "v_mfma_f32_16x16x32_bf16 v[84:87], a[28:31], a[68:71], v[84:87]\n"
    "v_mul_f32 v33, s72, v33\n"
    "v_mul_f32 v34, s72, v34\n"
    "v_mfma_f32_16x16x32_bf16 v[88:91], a[28:31], a[72:75], v[88:91]\n"
    "v_mul_f32 v35, s72, v35\n"
    "v_cvt_pk_bf16_f32 v32, v32, v33\n"
    "v_mfma_f32_16x16x32_bf16 v[92:95], a[28:31], a[76:79], v[92:95]\n"
    "v_cvt_pk_bf16_f32 v33, v34, v35\n"
    "ds_write_b64 v163, v[32:33] offset:16384\n"
    "v_mul_f32 v36, s72, v36\n"
    "v_mul_f32 v37, s72, v37\n"
    "v_mul_f32 v38, s72, v38\n"
    "v_mul_f32 v39, s72, v39\n"
    "v_cvt_pk_bf16_f32 v36, v36, v37\n"
    "v_cvt_pk_bf16_f32 v37, v38, v39\n"
    "ds_write_b64 v163, v[36:37] offset:24576\n"
    "v_mul_f32 v40, s72, v40\n"
    "v_mul_f32 v41, s72, v41\n"
    "v_mul_f32 v42, s72, v42\n"
    "v_mul_f32 v43, s72, v43\n"
    "v_cvt_pk_bf16_f32 v40, v40, v41\n"
    "v_cvt_pk_bf16_f32 v41, v42, v43\n"
    "ds_write_b64 v163, v[40:41] offset:32768\n"
    "v_pk_mul_f32 v[44:45], v[44:45], s[72:73]\n"
    "v_pk_mul_f32 v[46:47], v[46:47], s[72:73]\n"
    "v_cvt_pk_bf16_f32 v44, v44, v45\n"
    "v_cvt_pk_bf16_f32 v45, v46, v47\n"
    "ds_write_b64 v163, v[44:45] offset:40960\n"
    "v_pk_mul_f32 v[48:49], v[48:49], s[72:73]\n"
    "v_pk_mul_f32 v[50:51], v[50:51], s[72:73]\n"
    "v_cvt_pk_bf16_f32 v48, v48, v49\n"
    "v_cvt_pk_bf16_f32 v49, v50, v51\n"
    "ds_write_b64 v164, v[48:49] offset:0\n"
    "v_pk_mul_f32 v[52:53], v[52:53], s[72:73]\n"
    "v_pk_mul_f32 v[54:55], v[54:55], s[72:73]\n"
    "v_cvt_pk_bf16_f32 v52, v52, v53\n"
    "v_cvt_pk_bf16_f32 v53, v54, v55\n"
    "ds_write_b64 v164, v[52:53] offset:8192\n"
    "v_pk_mul_f32 v[56:57], v[56:57], s[72:73]\n"
    "v_pk_mul_f32 v[58:59], v[58:59], s[72:73]\n"
    "v_cvt_pk_bf16_f32 v56, v56, v57\n"
    "v_cvt_pk_bf16_f32 v57, v58, v59\n"
    "ds_write_b64 v164, v[56:57] offset:16384\n"
    "v_pk_mul_f32 v[60:61], v[60:61], s[72:73]\n"
    "v_pk_mul_f32 v[62:63], v[62:63], s[72:73]\n"
    "v_cvt_pk_bf16_f32 v60, v60, v61\n"
    "v_cvt_pk_bf16_f32 v61, v62, v63\n"
    "ds_write_b64 v164, v[60:61] offset:24576\n"
    "v_pk_mul_f32 v[64:65], v[64:65], s[72:73]\n"
    "v_pk_mul_f32 v[66:67], v[66:67], s[72:73]\n"
    "v_cvt_pk_bf16_f32 v64, v64, v65\n"
    "v_cvt_pk_bf16_f32 v65, v66, v67\n"
    "ds_write_b64 v164, v[64:65] offset:32768\n"
    "v_pk_mul_f32 v[68:69], v[68:69], s[72:73]\n"
    "v_pk_mul_f32 v[70:71], v[70:71], s[72:73]\n"
    "v_cvt_pk_bf16_f32 v68, v68, v69\n"
    "v_cvt_pk_bf16_f32 v69, v70, v71\n"
    "ds_write_b64 v164, v[68:69] offset:40960\n"
    "v_pk_mul_f32 v[72:73], v[72:73], s[72:73]\n"
    "v_pk_mul_f32 v[76:77], v[76:77], s[72:73]\n"
    "v_pk_mul_f32 v[80:81], v[80:81], s[72:73]\n"
    "v_pk_mul_f32 v[84:85], v[84:85], s[72:73]\n"
    "v_pk_mul_f32 v[88:89], v[88:89], s[72:73]\n"
    "v_pk_mul_f32 v[92:93], v[92:93], s[72:73]\n"
    "v_pk_mul_f32 v[74:75], v[74:75], s[72:73]\n"
    "v_pk_mul_f32 v[78:79], v[78:79], s[72:73]\n"
    "v_pk_mul_f32 v[82:83], v[82:83], s[72:73]\n"
    "v_pk_mul_f32 v[86:87], v[86:87], s[72:73]\n"
    "v_pk_mul_f32 v[90:91], v[90:91], s[72:73]\n"
    "v_pk_mul_f32 v[94:95], v[94:95], s[72:73]\n"
    "v_cvt_pk_bf16_f32 v72, v72, v73\n"
    "v_cvt_pk_bf16_f32 v76, v76, v77\n"
    "v_cvt_pk_bf16_f32 v80, v80, v81\n"
    "v_cvt_pk_bf16_f32 v84, v84, v85\n"
    "v_cvt_pk_bf16_f32 v88, v88, v89\n"
    "v_cvt_pk_bf16_f32 v92, v92, v93\n"
    "v_cvt_pk_bf16_f32 v73, v74, v75\n"
    "v_cvt_pk_bf16_f32 v77, v78, v79\n"
    "v_cvt_pk_bf16_f32 v81, v82, v83\n"
    "v_cvt_pk_bf16_f32 v85, v86, v87\n"
    "v_cvt_pk_bf16_f32 v89, v90, v91\n"
    "v_cvt_pk_bf16_f32 v93, v94, v95\n"
    "ds_write_b64 v165, v[72:73] offset:0\n"
    "ds_write_b64 v165, v[76:77] offset:8192\n"
    "ds_write_b64 v165, v[80:81] offset:16384\n"
    "ds_write_b64 v165, v[84:85] offset:24576\n"
    "ds_write_b64 v165, v[88:89] offset:32768\n"
    "ds_write_b64 v165, v[92:93] offset:40960\n"
    "s_waitcnt vmcnt(10) lgkmcnt(0)\n"
    "s_barrier\n"
    "ds_read_b128 a[96:99], v166 offset:0\n"
    "ds_read_b128 a[128:131], v166 offset:8192\n"
    "ds_read_b128 a[160:163], v166 offset:16384\n"
    "ds_read_b128 a[192:195], v188 offset:0\n"
    "ds_read_b128 a[196:199], v188 offset:8192\n"
    "ds_read_b128 a[100:103], v167 offset:0\n"
    "ds_read_b128 a[132:135], v167 offset:8192\n"
    "ds_read_b128 a[164:167], v167 offset:16384\n"
    "ds_read_b128 a[200:203], v189 offset:0\n"
    "ds_read_b128 a[204:207], v189 offset:8192\n"
    "s_waitcnt lgkmcnt(5)\n"
    "v_mfma_f32_16x16x32_bf16 v[96:99], a[192:195], a[96:99], 0\n"
    "ds_read_b128 a[104:107], v168 offset:0\n"
    "ds_read_b128 a[136:139], v168 offset:8192\n"
    "v_mfma_f32_16x16x32_bf16 v[100:103], a[192:195], a[128:131], 0\n"
    "buffer_load_dwordx4 v210, s[52:55], s67 offen offset:0 nt lds\n"
    "ds_read_b128 a[168:171], v168 offset:16384\n"
    "v_mfma_f32_16x16x32_bf16 v[104:107], a[192:195], a[160:163], 0\n"
    "ds_read_b128 a[208:211], v190 offset:0\n"
    "ds_read_b128 a[212:215], v190 offset:8192\n"
    "v_mfma_f32_16x16x32_bf16 v[108:111], a[196:199], a[96:99], 0\n"
    "buffer_load_dwordx4 v211, s[52:55], s67 offen offset:1024 nt lds\n"
    "s_add_u32 m0, s91, 81920\n"
    "v_mfma_f32_16x16x32_bf16 v[112:115], a[196:199], a[128:131], 0\n"
    "ds_read_b128 a[108:111], v169 offset:0\n"
    "ds_read_b128 a[140:143], v169 offset:8192\n"
    "v_mfma_f32_16x16x32_bf16 v[116:119], a[196:199], a[160:163], 0\n"
    "ds_read_b128 a[172:175], v169 offset:16384\n"
    "s_waitcnt lgkmcnt(8)\n"
    "v_mfma_f32_16x16x32_bf16 v[96:99], a[200:203], a[100:103], v[96:99]\n"
    "buffer_load_dwordx4 v208, s[52:55], s68 offen offset:0 nt lds\n"
    "ds_read_b128 a[216:219], v191 offset:0\n"
    "v_mfma_f32_16x16x32_bf16 v[100:103], a[200:203], a[132:135], v[100:103]\n"
    "ds_read_b128 a[220:223], v191 offset:8192\n"
    "v_mfma_f32_16x16x32_bf16 v[104:107], a[200:203], a[164:167], v[104:107]\n"
    "v_mfma_f32_16x16x32_bf16 v[108:111], a[204:207], a[100:103], v[108:111]\n"
    "buffer_load_dwordx4 v209, s[52:55], s68 offen offset:1024 nt lds\n"
    "s_add_u32 m0, s91, 90112\n"
    "v_mfma_f32_16x16x32_bf16 v[112:115], a[204:207], a[132:135], v[112:115]\n"
    "ds_read_b128 a[112:115], v166 offset:256\n"
    "ds_read_b128 a[144:147], v166 offset:8448\n"
    "v_mfma_f32_16x16x32_bf16 v[116:119], a[204:207], a[164:167], v[116:119]\n"
    "ds_read_b128 a[176:179], v166 offset:16640\n"
    "s_waitcnt lgkmcnt(8)\n"
    "v_mfma_f32_16x16x32_bf16 v[96:99], a[208:211], a[104:107], v[96:99]\n"
    "buffer_load_dwordx4 v210, s[52:55], s68 offen offset:0 nt lds\n"
    "ds_read_b128 a[192:195], v188 offset:256\n"
    "v_mfma_f32_16x16x32_bf16 v[100:103], a[208:211], a[136:139], v[100:103]\n"
    "ds_read_b128 a[196:199], v188 offset:8448\n"
    "v_mfma_f32_16x16x32_bf16 v[104:107], a[208:211], a[168:171], v[104:107]\n"
    "v_mfma_f32_16x16x32_bf16 v[108:111], a[212:215], a[104:107], v[108:111]\n"
    "buffer_load_dwordx4 v211, s[52:55], s68 offen offset:1024 nt lds\n"
    "s_add_u32 s67, s67, 32768\n"
    "v_mfma_f32_16x16x32_bf16 v[112:115], a[212:215], a[136:139], v[112:115]\n"
    "ds_read_b128 a[116:119], v167 offset:256\n"
    "s_add_u32 s68, s68, 32768\n"
    "v_mfma_f32_16x16x32_bf16 v[116:119], a[212:215], a[168:171], v[116:119]\n"
    "ds_read_b128 a[148:151], v167 offset:8448\n"
    "s_waitcnt lgkmcnt(7)\n"
    "v_mfma_f32_16x16x32_bf16 v[96:99], a[216:219], a[108:111], v[96:99]\n"
    "ds_read_b128 a[180:183], v167 offset:16640\n"
    "ds_read_b128 a[200:203], v189 offset:256\n"
    "v_mfma_f32_16x16x32_bf16 v[100:103], a[216:219], a[140:143], v[100:103]\n"
    "ds_read_b128 a[204:207], v189 offset:8448\n"
    "v_mfma_f32_16x16x32_bf16 v[104:107], a[216:219], a[172:175], v[104:107]\n"
    "v_mfma_f32_16x16x32_bf16 v[108:111], a[220:223], a[108:111], v[108:111]\n"
    "v_mfma_f32_16x16x32_bf16 v[112:115], a[220:223], a[140:143], v[112:115]\n"
    "ds_read_b128 a[120:123], v168 offset:256\n"
    "ds_read_b128 a[152:155], v168 offset:8448\n"
    "v_mfma_f32_16x16x32_bf16 v[116:119], a[220:223], a[172:175], v[116:119]\n"
    "ds_read_b128 a[184:187], v168 offset:16640\n"
    "s_waitcnt lgkmcnt(8)\n"
    "v_mfma_f32_16x16x32_bf16 v[96:99], a[192:195], a[112:115], v[96:99]\n"
    "ds_read_b128 a[208:211], v190 offset:256\n"
    "ds_read_b128 a[212:215], v190 offset:8448\n"
    "v_mfma_f32_16x16x32_bf16 v[100:103], a[192:195], a[144:147], v[100:103]\n"
    "v_mfma_f32_16x16x32_bf16 v[104:107], a[192:195], a[176:179], v[104:107]\n"
    "v_mfma_f32_16x16x32_bf16 v[108:111], a[196:199], a[112:115], v[108:111]\n"
    "v_mfma_f32_16x16x32_bf16 v[112:115], a[196:199], a[144:147], v[112:115]\n"
    "ds_read_b128 a[124:127], v169 offset:256\n"
    "ds_read_b128 a[156:159], v169 offset:8448\n"
    "v_mfma_f32_16x16x32_bf16 v[116:119], a[196:199], a[176:179], v[116:119]\n"
    "ds_read_b128 a[188:191], v169 offset:16640\n"
    "s_waitcnt lgkmcnt(8)\n"
    "v_mfma_f32_16x16x32_bf16 v[96:99], a[200:203], a[116:119], v[96:99]\n"
    "ds_read_b128 a[216:219], v191 offset:256\n"
    "ds_read_b128 a[220:223], v191 offset:8448\n"
    "v_mfma_f32_16x16x32_bf16 v[100:103], a[200:203], a[148:151], v[100:103]\n"
    "v_mfma_f32_16x16x32_bf16 v[104:107], a[200:203], a[180:183], v[104:107]\n"
    "v_mfma_f32_16x16x32_bf16 v[108:111], a[204:207], a[116:119], v[108:111]\n"
    "v_mfma_f32_16x16x32_bf16 v[112:115], a[204:207], a[148:151], v[112:115]\n"
    "v_mfma_f32_16x16x32_bf16 v[116:119], a[204:207], a[180:183], v[116:119]\n"
    "s_waitcnt lgkmcnt(5)\n"
    "v_mfma_f32_16x16x32_bf16 v[96:99], a[208:211], a[120:123], v[96:99]\n"
    "s_mul_i32 s76, s64, 3072\n"
    "s_add_u32 s76, s76, 131072\n"
    "v_mfma_f32_16x16x32_bf16 v[100:103], a[208:211], a[152:155], v[100:103]\n"
    "v_lshl_add_u32 v172, v254, 4, s76\n"
    "s_and_b32 s76, s64, 1\n"
    "v_mfma_f32_16x16x32_bf16 v[104:107], a[208:211], a[184:187], v[104:107]\n"
    "s_mul_i32 s76, s76, 3072\n"
    "s_add_u32 s76, s76, 131072\n"
    "v_mfma_f32_16x16x32_bf16 v[108:111], a[212:215], a[120:123], v[108:111]\n"
    "v_lshl_add_u32 v173, v254, 4, s76\n"
    "v_add_u32 v175, 155648, v250\n"
    "v_mfma_f32_16x16x32_bf16 v[112:115], a[212:215], a[152:155], v[112:115]\n"
    "s_waitcnt vmcnt(8) lgkmcnt(0)\n"
    "s_barrier\n"
    "v_mfma_f32_16x16x32_bf16 v[116:119], a[212:215], a[184:187], v[116:119]\n"
    "ds_read_b128 a[192:195], v188 offset:32768\n"
    "v_mfma_f32_16x16x32_bf16 v[96:99], a[216:219], a[124:127], v[96:99]\n"
    "ds_read_b128 a[196:199], v188 offset:40960\n"
    "ds_read_b128 a[200:203], v189 offset:32768\n"
    "v_mfma_f32_16x16x32_bf16 v[100:103], a[216:219], a[156:159], v[100:103]\n"
    "ds_read_b128 a[204:207], v189 offset:40960\n"
    "s_lshl_b32 s76, s64, 2\n"
    "v_mfma_f32_16x16x32_bf16 v[104:107], a[216:219], a[188:191], v[104:107]\n"
    "v_add_u32 v174, s76, v175\n"
    "v_mfma_f32_16x16x32_bf16 v[108:111], a[220:223], a[124:127], v[108:111]\n"
    "v_mfma_f32_16x16x32_bf16 v[112:115], a[220:223], a[156:159], v[112:115]\n"
    "v_mfma_f32_16x16x32_bf16 v[116:119], a[220:223], a[188:191], v[116:119]\n"
    "s_nop 5\n"
    "v_mov_b32 v174, 0\n"
    "ds_write_b32 v175, v174 offset:0\n"
    "v_max3_f32 v232, v96, v97, v98\n"
    "v_max3_f32 v233, v100, v101, v102\n"
    "v_max3_f32 v234, v104, v105, v106\n"
    "v_max3_f32 v232, v232, v99, v108\n"
    "v_max3_f32 v232, v232, v109, v110\n"
    "v_max_f32 v232, v232, v111\n"
    "v_max3_f32 v233, v233, v103, v112\n"
    "v_max3_f32 v233, v233, v113, v114\n"
    "v_max_f32 v233, v233, v115\n"
    "v_max3_f32 v234, v234, v107, v116\n"
    "v_max3_f32 v234, v234, v117, v118\n"
    "v_max_f32 v234, v234, v119\n"
    "v_mov_b32 v235, v232\n"
    "v_mov_b32 v236, v233\n"
    "v_mov_b32 v237, v234\n"
    "s_nop 1\n"
    "v_permlane16_swap_b32 v232, v235\n"
    "v_permlane16_swap_b32 v233, v236\n"
    "v_permlane16_swap_b32 v234, v237\n"
    "s_nop 0\n"
    "v_max_f32 v232, v232, v235\n"
    "v_max_f32 v233, v233, v236\n"
    "v_max_f32 v234, v234, v237\n"
    "v_mov_b32 v235, v232\n"
    "v_mov_b32 v236, v233\n"
    "v_mov_b32 v237, v234\n"
    "s_nop 1\n"
    "v_permlane32_swap_b32 v232, v235\n"
    "v_permlane32_swap_b32 v233, v236\n"
    "v_permlane32_swap_b32 v234, v237\n"
    "s_nop 0\n"
    "v_max_f32 v232, v232, v235\n"
    "v_max_f32 v233, v233, v236\n"
    "v_max_f32 v234, v234, v237\n"
    "ds_write_b32 v251, v232 offset:0\n"
    "ds_write_b32 v251, v233 offset:64\n"
    "ds_write_b32 v251, v234 offset:128\n"
    "s_waitcnt lgkmcnt(0)\n"
    "s_barrier\n"
    "ds_read_b32 v235, v252 offset:0\n"
    "ds_read_b32 v236, v252 offset:64\n"
    "ds_read_b32 v237, v252 offset:128\n"
    "s_waitcnt lgkmcnt(0)\n"
    "v_max_f32 v232, v232, v235\n"
    "v_max_f32 v233, v233, v236\n"
    "v_max_f32 v234, v234, v237\n"
    "v_sub_f32 v144, 0, v232\n"
    "v_sub_f32 v145, 0, v232\n"
    "v_sub_f32 v146, 0, v232\n"
    "v_sub_f32 v147, 0, v232\n"
    "v_sub_f32 v148, 0, v233\n"
    "v_sub_f32 v149, 0, v233\n"
    "v_sub_f32 v150, 0, v233\n"
    "v_sub_f32 v151, 0, v233\n"
    "v_sub_f32 v152, 0, v234\n"
    "v_sub_f32 v153, 0, v234\n"
    "v_sub_f32 v154, 0, v234\n"
    "v_sub_f32 v155, 0, v234\n"
    "s_nop 0\n"
    "v_mfma_f32_16x16x32_bf16 v[120:123], a[192:195], a[96:99], v[144:147]\n"
    "s_add_u32 m0, s91, 0\n"
    "s_nop 0\n"
    "buffer_load_dwordx4 v200, s[48:51], s66 offen offset:0 nt lds\n"
    "ds_read_b128 a[208:211], v190 offset:32768\n"
    "ds_read_b128 a[212:215], v190 offset:40960\n"
    "v_mfma_f32_16x16x32_bf16 v[124:127], a[192:195], a[128:131], v[148:151]\n"
    "ds_read_b128 a[216:219], v191 offset:32768\n"
    "v_add_f32 v96, v96, v144\n"
    "v_exp_f32 v180, v96\n"
    "v_mfma_f32_16x16x32_bf16 v[128:131], a[192:195], a[160:163], v[152:155]\n"
    "ds_read_b128 a[220:223], v191 offset:40960\n"
    "v_add_f32 v97, v97, v144\n"
    "v_exp_f32 v181, v97\n"
    "v_mfma_f32_16x16x32_bf16 v[132:135], a[196:199], a[96:99], v[144:147]\n"
    "buffer_load_dwordx4 v201, s[48:51], s66 offen offset:1024 nt lds\n"
    "v_add_f32 v98, v98, v144\n"
    "v_exp_f32 v182, v98\n"
    "v_mfma_f32_16x16x32_bf16 v[136:139], a[196:199], a[128:131], v[148:151]\n"
    "s_add_u32 m0, s91, 8192\n"
    "v_add_f32 v99, v99, v144\n"
    "v_exp_f32 v183, v99\n"
    "v_mfma_f32_16x16x32_bf16 v[140:143], a[196:199], a[160:163], v[152:155]\n"
    "v_add_f32 v108, v108, v144\n"
    "v_exp_f32 v184, v108\n"
    "v_mfma_f32_16x16x32_bf16 v[120:123], a[200:203], a[100:103], v[120:123]\n"
    "buffer_load_dwordx4 v202, s[48:51], s66 offen offset:0 nt lds\n"
    "v_add_f32 v109, v109, v144\n"
    "v_add_f32 v110, v110, v144\n"
    "v_add_f32 v111, v111, v144\n"
    "v_mfma_f32_16x16x32_bf16 v[124:127], a[200:203], a[132:135], v[124:127]\n"
    "v_exp_f32 v185, v109\n"
    "v_cvt_pk_bf16_f32 v156, v180, v181\n"
    "v_add_f32 v215, v180, v181\n"
    "v_mfma_f32_16x16x32_bf16 v[128:131], a[200:203], a[164:167], v[128:131]\n"
    "v_exp_f32 v186, v110\n"
    "v_cvt_pk_bf16_f32 v157, v182, v183\n"
    "v_add_f32 v215, v215, v182\n"
    "v_mfma_f32_16x16x32_bf16 v[132:135], a[204:207], a[100:103], v[132:135]\n"
    "buffer_load_dwordx4 v203, s[48:51], s66 offen offset:1024 nt lds\n"
    "v_add_f32 v215, v215, v183\n"
    "s_add_u32 m0, s91, 16384\n"
    "v_add_f32 v100, v100, v148\n"
    "v_mfma_f32_16x16x32_bf16 v[136:139], a[204:207], a[132:135], v[136:139]\n"
    "v_exp_f32 v187, v111\n"
    "v_exp_f32 v180, v100\n"
    "v_mfma_f32_16x16x32_bf16 v[140:143], a[204:207], a[164:167], v[140:143]\n"
    "v_add_f32 v101, v101, v148\n"
    "v_add_f32 v102, v102, v148\n"
    "ds_read_b128 a[192:195], v188 offset:33024\n"
    "s_waitcnt lgkmcnt(1)\n"
    "v_mfma_f32_16x16x32_bf16 v[120:123], a[208:211], a[104:107], v[120:123]\n"
    "buffer_load_dwordx4 v204, s[48:51], s66 offen offset:0 nt lds\n"
    "v_add_f32 v103, v103, v148\n"
    "v_cvt_pk_bf16_f32 v158, v184, v185\n"
    "ds_read_b128 a[196:199], v188 offset:41216\n"
    "v_mfma_f32_16x16x32_bf16 v[124:127], a[208:211], a[136:139], v[124:127]\n"
    "v_exp_f32 v181, v101\n"
    "v_add_f32 v215, v215, v184\n"
    "ds_read_b128 a[200:203], v189 offset:33024\n"
    "v_mfma_f32_16x16x32_bf16 v[128:131], a[208:211], a[168:171], v[128:131]\n"
    "v_exp_f32 v182, v102\n"
    "v_add_f32 v215, v215, v185\n"
    "v_cvt_pk_bf16_f32 v159, v186, v187\n"
    "v_mfma_f32_16x16x32_bf16 v[132:135], a[212:215], a[104:107], v[132:135]\n"
    "buffer_load_dwordx4 v205, s[48:51], s66 offen offset:1024 nt lds\n"
    "ds_read_b128 a[204:207], v189 offset:41216\n"
    "v_add_f32 v215, v215, v186\n"
    "ds_write_b128 v172, v[156:159] offset:0\n"
    "v_mfma_f32_16x16x32_bf16 v[136:139], a[212:215], a[136:139], v[136:139]\n"
    "s_add_u32 m0, s91, 24576\n"
    "v_exp_f32 v183, v103\n"
    "v_add_f32 v215, v215, v187\n"
    "v_mfma_f32_16x16x32_bf16 v[140:143], a[212:215], a[168:171], v[140:143]\n"
    "v_add_f32 v112, v112, v148\n"
    "v_exp_f32 v184, v112\n"
    "v_add_f32 v113, v113, v148\n"
    "v_mfma_f32_16x16x32_bf16 v[120:123], a[216:219], a[108:111], v[120:123]\n"
    "buffer_load_dwordx4 v206, s[48:51], s66 offen offset:0 nt lds\n"
    "v_add_f32 v114, v114, v148\n"
    "v_add_f32 v115, v115, v148\n"
    "v_cvt_pk_bf16_f32 v160, v180, v181\n"
    "v_mfma_f32_16x16x32_bf16 v[124:127], a[216:219], a[140:143], v[124:127]\n"
    "v_exp_f32 v185, v113\n"
    "v_exp_f32 v186, v114\n"
    "v_mfma_f32_16x16x32_bf16 v[128:131], a[216:219], a[172:175], v[128:131]\n"
    "v_add_f32 v216, v180, v181\n"
    "v_cvt_pk_bf16_f32 v161, v182, v183\n"
    "v_add_f32 v216, v216, v182\n"
    "v_add_f32 v216, v216, v183\n"
    "v_mfma_f32_16x16x32_bf16 v[132:135], a[220:223], a[108:111], v[132:135]\n"
    "buffer_load_dwordx4 v207, s[48:51], s66 offen offset:1024 nt lds\n"
    "s_add_u32 s66, s66, 32768\n"
    "s_add_u32 m0, s91, 98304\n"
    "v_add_f32 v104, v104, v152\n"
    "v_mfma_f32_16x16x32_bf16 v[136:139], a[220:223], a[140:143], v[136:139]\n"
    "v_exp_f32 v187, v115\n"
    "v_exp_f32 v180, v104\n"
    "v_mfma_f32_16x16x32_bf16 v[140:143], a[220:223], a[172:175], v[140:143]\n"
    "v_add_f32 v105, v105, v152\n"
    "v_add_f32 v106, v106, v152\n"
    "ds_read_b128 a[208:211], v190 offset:33024\n"
    "s_waitcnt lgkmcnt(2)\n"
    "v_mfma_f32_16x16x32_bf16 v[120:123], a[192:195], a[112:115], v[120:123]\n"
    "buffer_load_dwordx4 v208, s[52:55], s67 offen offset:0 nt lds\n"
    "v_add_f32 v107, v107, v152\n"
    "v_cvt_pk_bf16_f32 v162, v184, v185\n"
    "ds_read_b128 a[212:215], v190 offset:41216\n"
    "v_mfma_f32_16x16x32_bf16 v[124:127], a[192:195], a[144:147], v[124:127]\n"
    "v_exp_f32 v181, v105\n"
    "v_add_f32 v216, v216, v184\n"
    "ds_read_b128 a[216:219], v191 offset:33024\n"
    "v_mfma_f32_16x16x32_bf16 v[128:131], a[192:195], a[176:179], v[128:131]\n"
    "v_exp_f32 v182, v106\n"
    "v_add_f32 v216, v216, v185\n"
    "v_cvt_pk_bf16_f32 v163, v186, v187\n"
    "v_mfma_f32_16x16x32_bf16 v[132:135], a[196:199], a[112:115], v[132:135]\n"
    "buffer_load_dwordx4 v209, s[52:55], s67 offen offset:1024 nt lds\n"
    "ds_read_b128 a[220:223], v191 offset:41216\n"
    "v_add_f32 v216, v216, v186\n"
    "ds_write_b128 v172, v[160:163] offset:1024\n"
    "v_mfma_f32_16x16x32_bf16 v[136:139], a[196:199], a[144:147], v[136:139]\n"
    "s_add_u32 m0, s91, 106496\n"
    "v_exp_f32 v183, v107\n"
    "v_add_f32 v216, v216, v187\n"
    "v_mfma_f32_16x16x32_bf16 v[140:143], a[196:199], a[176:179], v[140:143]\n"
    "v_add_f32 v116, v116, v152\n"
    "v_exp_f32 v184, v116\n"
    "v_add_f32 v117, v117, v152\n"
    "v_mfma_f32_16x16x32_bf16 v[120:123], a[200:203], a[116:119], v[120:123]\n"
    "buffer_load_dwordx4 v210, s[52:55], s67 offen offset:0 nt lds\n"
    "v_add_f32 v118, v118, v152\n"
    "v_add_f32 v119, v119, v152\n"
    "v_cvt_pk_bf16_f32 v164, v180, v181\n"
    "v_mfma_f32_16x16x32_bf16 v[124:127], a[200:203], a[148:151], v[124:127]\n"
    "v_exp_f32 v185, v117\n"
    "v_exp_f32 v186, v118\n"
    "v_mfma_f32_16x16x32_bf16 v[128:131], a[200:203], a[180:183], v[128:131]\n"
    "v_add_f32 v217, v180, v181\n"
    "v_cvt_pk_bf16_f32 v165, v182, v183\n"
    "v_add_f32 v217, v217, v182\n"
    "v_add_f32 v217, v217, v183\n"
    "v_mfma_f32_16x16x32_bf16 v[132:135], a[204:207], a[116:119], v[132:135]\n"
    "buffer_load_dwordx4 v211, s[52:55], s67 offen offset:1024 nt lds\n"
    "s_add_u32 m0, s91, 114688\n"
    "v_exp_f32 v187, v119\n"
    "v_mfma_f32_16x16x32_bf16 v[136:139], a[204:207], a[148:151], v[136:139]\n"
    "v_cvt_pk_bf16_f32 v166, v184, v185\n"
    "v_add_f32 v217, v217, v184\n"
    "v_add_f32 v217, v217, v185\n"
    "v_cvt_pk_bf16_f32 v167, v186, v187\n"
    "v_mfma_f32_16x16x32_bf16 v[140:143], a[204:207], a[180:183], v[140:143]\n"
    "v_add_f32 v217, v217, v186\n"
    "ds_write_b128 v172, v[164:167] offset:2048\n"
    "v_add_f32 v217, v217, v187\n"
    "s_waitcnt lgkmcnt(2)\n"
    "v_mfma_f32_16x16x32_bf16 v[120:123], a[208:211], a[120:123], v[120:123]\n"
    "buffer_load_dwordx4 v208, s[52:55], s68 offen offset:0 nt lds\n"
    "v_mfma_f32_16x16x32_bf16 v[124:127], a[208:211], a[152:155], v[124:127]\n"
    "v_mfma_f32_16x16x32_bf16 v[128:131], a[208:211], a[184:187], v[128:131]\n"
    "v_mfma_f32_16x16x32_bf16 v[132:135], a[212:215], a[120:123], v[132:135]\n"
    "buffer_load_dwordx4 v209, s[52:55], s68 offen offset:1024 nt lds\n"
    "s_add_u32 m0, s91, 122880\n"
    "v_mfma_f32_16x16x32_bf16 v[136:139], a[212:215], a[152:155], v[136:139]\n"
    "v_mfma_f32_16x16x32_bf16 v[140:143], a[212:215], a[184:187], v[140:143]\n"
    "v_mfma_f32_16x16x32_bf16 v[120:123], a[216:219], a[124:127], v[120:123]\n"
    "buffer_load_dwordx4 v210, s[52:55], s68 offen offset:0 nt lds\n"
    "s_waitcnt vmcnt(15) lgkmcnt(0)\n"
    "s_barrier\n"
    "v_mfma_f32_16x16x32_bf16 v[124:127], a[216:219], a[156:159], v[124:127]\n"
    "ds_read_b128 a[0:3], v173 offset:0\n"
    "ds_read_b128 a[4:7], v173 offset:1024\n"
    "ds_read_b128 a[8:11], v173 offset:2048\n"
    "ds_read_b64_tr_b16 a[224:225], v192 offset:0\n"
    "v_mfma_f32_16x16x32_bf16 v[128:131], a[216:219], a[188:191], v[128:131]\n"
    "ds_read_b64_tr_b16 a[226:227], v192 offset:8192\n"
    "ds_read_b64_tr_b16 a[228:229], v193 offset:0\n"
    "ds_read_b64_tr_b16 a[230:231], v193 offset:8192\n"
    "ds_read_b64_tr_b16 a[232:233], v194 offset:0\n"
    "v_mfma_f32_16x16x32_bf16 v[132:135], a[220:223], a[124:127], v[132:135]\n"
    "ds_read_b64_tr_b16 a[234:235], v194 offset:8192\n"
    "ds_read_b64_tr_b16 a[236:237], v195 offset:0\n"
    "ds_read_b64_tr_b16 a[238:239], v195 offset:8192\n"
    "buffer_load_dwordx4 v211, s[52:55], s68 offen offset:1024 nt lds\n"
    "v_mfma_f32_16x16x32_bf16 v[136:139], a[220:223], a[156:159], v[136:139]\n"
    "s_add_u32 s67, s67, 32768\n"
    "s_add_u32 s68, s68, 32768\n"
    "v_mfma_f32_16x16x32_bf16 v[140:143], a[220:223], a[188:191], v[140:143]\n"
    "s_waitcnt lgkmcnt(4)\n"
    "v_mfma_f32_16x16x32_bf16 v[0:3], a[224:227], a[0:3], 0\n"
    "s_add_u32 m0, s91, 32768\n"
    "ds_read_b64_tr_b16 a[240:241], v196 offset:0\n"
    "v_mfma_f32_16x16x32_bf16 v[4:7], a[224:227], a[4:7], 0\n"
    "ds_read_b64_tr_b16 a[242:243], v196 offset:8192\n"
    "buffer_load_dwordx4 v200, s[48:51], s66 offen offset:0 nt lds\n"
    "v_mfma_f32_16x16x32_bf16 v[8:11], a[224:227], a[8:11], 0\n"
    "ds_read_b64_tr_b16 a[244:245], v197 offset:0\n"
    "ds_read_b64_tr_b16 a[246:247], v197 offset:8192\n"
    "v_mfma_f32_16x16x32_bf16 v[12:15], a[228:231], a[0:3], 0\n"
    "ds_read_b64_tr_b16 a[248:249], v198 offset:0\n"
    "ds_read_b64_tr_b16 a[250:251], v198 offset:8192\n"
    "v_mfma_f32_16x16x32_bf16 v[16:19], a[228:231], a[4:7], 0\n"
    "v_exp_f32 v180, v120\n"
    "v_mfma_f32_16x16x32_bf16 v[20:23], a[228:231], a[8:11], 0\n"
    "ds_read_b128 a[12:15], v173 offset:6144\n"
    "ds_read_b128 a[16:19], v173 offset:7168\n"
    "ds_read_b128 a[20:23], v173 offset:8192\n"
    "ds_read_b64_tr_b16 a[252:253], v199 offset:0\n"
    "ds_read_b64_tr_b16 a[254:255], v199 offset:8192\n"
    "s_waitcnt lgkmcnt(11)\n"
    "v_mfma_f32_16x16x32_bf16 v[24:27], a[232:235], a[0:3], 0\n"
    "v_add_f32 v212, v212, v215\n"
    "v_add_f32 v213, v213, v216\n"
    "v_mfma_f32_16x16x32_bf16 v[28:31], a[232:235], a[4:7], 0\n"
    "buffer_load_dwordx4 v201, s[48:51], s66 offen offset:1024 nt lds\n"
    "v_add_f32 v214, v214, v217\n"
    "v_mfma_f32_16x16x32_bf16 v[32:35], a[232:235], a[8:11], 0\n"
    "v_exp_f32 v181, v121\n"
    "v_mfma_f32_16x16x32_bf16 v[36:39], a[236:239], a[0:3], 0\n"
    "v_exp_f32 v182, v122\n"
    "v_mfma_f32_16x16x32_bf16 v[40:43], a[236:239], a[4:7], 0\n"
    "s_add_u32 m0, s91, 40960\n"
    "v_cvt_pk_bf16_f32 v156, v180, v181\n"
    "v_mfma_f32_16x16x32_bf16 v[44:47], a[236:239], a[8:11], 0\n"
    "ds_read_b64_tr_b16 a[224:225], v192 offset:16384\n"
    "s_waitcnt lgkmcnt(1)\n"
    "v_mfma_f32_16x16x32_bf16 v[48:51], a[240:243], a[0:3], 0\n"
    "ds_read_b64_tr_b16 a[226:227], v192 offset:24576\n"
    "ds_read_b64_tr_b16 a[228:229], v193 offset:16384\n"
    "v_mfma_f32_16x16x32_bf16 v[52:55], a[240:243], a[4:7], 0\n"
    "buffer_load_dwordx4 v202, s[48:51], s66 offen offset:0 nt lds\n"
    "ds_read_b64_tr_b16 a[230:231], v193 offset:24576\n"
    "v_mfma_f32_16x16x32_bf16 v[56:59], a[240:243], a[8:11], 0\n"
    "v_exp_f32 v183, v123\n"
    "v_mfma_f32_16x16x32_bf16 v[60:63], a[244:247], a[0:3], 0\n"
    "v_add_f32 v218, v180, v181\n"
    "ds_read_b64_tr_b16 a[232:233], v194 offset:16384\n"
    "v_mfma_f32_16x16x32_bf16 v[64:67], a[244:247], a[4:7], 0\n"
    "ds_read_b64_tr_b16 a[234:235], v194 offset:24576\n"
    "ds_read_b64_tr_b16 a[236:237], v195 offset:16384\n"
    "v_mfma_f32_16x16x32_bf16 v[68:71], a[244:247], a[8:11], 0\n"
    "ds_read_b64_tr_b16 a[238:239], v195 offset:24576\n"
    "v_cvt_pk_bf16_f32 v157, v182, v183\n"
    "v_mfma_f32_16x16x32_bf16 v[72:75], a[248:251], a[0:3], 0\n"
    "v_exp_f32 v184, v132\n"
    "v_mfma_f32_16x16x32_bf16 v[76:79], a[248:251], a[4:7], 0\n"
    "v_add_f32 v218, v218, v182\n"
    "buffer_load_dwordx4 v203, s[48:51], s66 offen offset:1024 nt lds\n"
    "v_mfma_f32_16x16x32_bf16 v[80:83], a[248:251], a[8:11], 0\n"
    "v_add_f32 v218, v218, v183\n"
    "ds_read_b64_tr_b16 a[240:241], v196 offset:16384\n"
    "v_mfma_f32_16x16x32_bf16 v[84:87], a[252:255], a[0:3], 0\n"
    "v_exp_f32 v185, v133\n"
    "v_mfma_f32_16x16x32_bf16 v[88:91], a[252:255], a[4:7], 0\n"
    "s_add_u32 m0, s91, 49152\n"
    "ds_read_b64_tr_b16 a[242:243], v196 offset:24576\n"
    "v_mfma_f32_16x16x32_bf16 v[92:95], a[252:255], a[8:11], 0\n"
    "ds_read_b64_tr_b16 a[244:245], v197 offset:16384\n"
    "s_waitcnt lgkmcnt(3)\n"
    "v_mfma_f32_16x16x32_bf16 v[0:3], a[224:227], a[12:15], v[0:3]\n"
    "v_exp_f32 v186, v134\n"
    "v_cvt_pk_bf16_f32 v158, v184, v185\n"
    "v_mfma_f32_16x16x32_bf16 v[4:7], a[224:227], a[16:19], v[4:7]\n"
    "buffer_load_dwordx4 v204, s[48:51], s66 offen offset:0 nt lds\n"
    "v_add_f32 v218, v218, v184\n"
    "v_mfma_f32_16x16x32_bf16 v[8:11], a[224:227], a[20:23], v[8:11]\n"
    "ds_read_b64_tr_b16 a[246:247], v197 offset:24576\n"
    "ds_read_b64_tr_b16 a[248:249], v198 offset:16384\n"
    "v_mfma_f32_16x16x32_bf16 v[12:15], a[228:231], a[12:15], v[12:15]\n"
    "ds_read_b64_tr_b16 a[250:251], v198 offset:24576\n"
    "v_add_f32 v218, v218, v185\n"
    "v_mfma_f32_16x16x32_bf16 v[16:19], a[228:231], a[16:19], v[16:19]\n"
    "ds_read_b64_tr_b16 a[252:253], v199 offset:16384\n"
    "ds_read_b64_tr_b16 a[254:255], v199 offset:24576\n"
    "v_mfma_f32_16x16x32_bf16 v[20:23], a[228:231], a[20:23], v[20:23]\n"
    "v_exp_f32 v187, v135\n"
    "v_mfma_f32_16x16x32_bf16 v[24:27], a[232:235], a[12:15], v[24:27]\n"
    "v_cvt_pk_bf16_f32 v159, v186, v187\n"
    "v_add_f32 v218, v218, v186\n"
    "v_mfma_f32_16x16x32_bf16 v[28:31], a[232:235], a[16:19], v[28:31]\n"
    "buffer_load_dwordx4 v205, s[48:51], s66 offen offset:1024 nt lds\n"
    "ds_write_b128 v172, v[156:159] offset:12288\n"
    "v_mfma_f32_16x16x32_bf16 v[32:35], a[232:235], a[20:23], v[32:35]\n"
    "v_exp_f32 v180, v124\n"
    "v_mfma_f32_16x16x32_bf16 v[36:39], a[236:239], a[12:15], v[36:39]\n"
    "v_exp_f32 v181, v125\n"
    "v_mfma_f32_16x16x32_bf16 v[40:43], a[236:239], a[16:19], v[40:43]\n"
    "v_add_f32 v218, v218, v187\n"
    "s_add_u32 m0, s91, 57344\n"
    "v_mfma_f32_16x16x32_bf16 v[44:47], a[236:239], a[20:23], v[44:47]\n"
    "s_waitcnt lgkmcnt(1)\n"
    "v_mfma_f32_16x16x32_bf16 v[48:51], a[240:243], a[12:15], v[48:51]\n"
    "v_exp_f32 v182, v126\n"
    "v_mfma_f32_16x16x32_bf16 v[52:55], a[240:243], a[16:19], v[52:55]\n"
    "buffer_load_dwordx4 v206, s[48:51], s66 offen offset:0 nt lds\n"
    "v_cvt_pk_bf16_f32 v160, v180, v181\n"
    "v_mfma_f32_16x16x32_bf16 v[56:59], a[240:243], a[20:23], v[56:59]\n"
    "v_add_f32 v219, v180, v181\n"
    "v_mfma_f32_16x16x32_bf16 v[60:63], a[244:247], a[12:15], v[60:63]\n"
    "v_exp_f32 v183, v127\n"
    "v_mfma_f32_16x16x32_bf16 v[64:67], a[244:247], a[16:19], v[64:67]\n"
    "s_waitcnt vmcnt(15) lgkmcnt(0)\n"
    "s_barrier\n"
    "v_mfma_f32_16x16x32_bf16 v[68:71], a[244:247], a[20:23], v[68:71]\n"
    "ds_read_b128 a[192:195], v188 offset:0\n"
    "ds_read_b128 a[196:199], v188 offset:8192\n"
    "v_mfma_f32_16x16x32_bf16 v[72:75], a[248:251], a[12:15], v[72:75]\n"
    "v_cvt_pk_bf16_f32 v161, v182, v183\n"
    "ds_read_b128 a[200:203], v189 offset:0\n"
    "v_mfma_f32_16x16x32_bf16 v[76:79], a[248:251], a[16:19], v[76:79]\n"
    "ds_read_b128 a[204:207], v189 offset:8192\n"
    "buffer_load_dwordx4 v207, s[48:51], s66 offen offset:1024 nt lds\n"
    "v_mfma_f32_16x16x32_bf16 v[80:83], a[248:251], a[20:23], v[80:83]\n"
    "v_add_f32 v219, v219, v182\n"
    "s_add_u32 s66, s66, 32768\n"
    "v_mfma_f32_16x16x32_bf16 v[84:87], a[252:255], a[12:15], v[84:87]\n"
    "v_add_f32 v219, v219, v183\n"
    "v_mfma_f32_16x16x32_bf16 v[88:91], a[252:255], a[16:19], v[88:91]\n"
    "v_mfma_f32_16x16x32_bf16 v[92:95], a[252:255], a[20:23], v[92:95]\n"
    "s_waitcnt lgkmcnt(2)\n"
    "s_mov_b32 s69, 5\n"
    ".Lal0_%=:\n"
    "v_mfma_f32_16x16x32_bf16 v[96:99], a[192:195], a[96:99], v[144:147]\n"
    "s_add_u32 m0, s91, 65536\n"
    "ds_read_b128 a[208:211], v190 offset:0\n"
    "v_mfma_f32_16x16x32_bf16 v[100:103], a[192:195], a[128:131], v[148:151]\n"
    "s_cmp_eq_u32 s69, 0\n"
    "s_cbranch_scc1 .Lonce_%=\n"
    ".Lonceb_%=:\n"
    "v_mfma_f32_16x16x32_bf16 v[104:107], a[192:195], a[160:163], v[152:155]\n"
    "buffer_load_dwordx4 v208, s[52:55], s67 offen offset:0 nt lds\n"
    "v_exp_f32 v184, v136\n"
    "v_mfma_f32_16x16x32_bf16 v[108:111], a[196:199], a[96:99], v[144:147]\n"
    "ds_read_b128 a[212:215], v190 offset:8192\n"
    "ds_read_b128 a[216:219], v191 offset:0\n"
    "v_mfma_f32_16x16x32_bf16 v[112:115], a[196:199], a[128:131], v[148:151]\n"
    "ds_read_b128 a[220:223], v191 offset:8192\n"
    "ds_read_b128 a[192:195], v188 offset:256\n"
    "v_mfma_f32_16x16x32_bf16 v[116:119], a[196:199], a[160:163], v[152:155]\n"
    "v_exp_f32 v185, v137\n"
    "s_waitcnt lgkmcnt(5)\n"
    "v_mfma_f32_16x16x32_bf16 v[96:99], a[200:203], a[100:103], v[96:99]\n"
    "v_exp_f32 v186, v138\n"
    "v_cvt_pk_bf16_f32 v162, v184, v185\n"
    "v_mfma_f32_16x16x32_bf16 v[100:103], a[200:203], a[132:135], v[100:103]\n"
    "v_add_f32 v219, v219, v184\n"
    "buffer_load_dwordx4 v209, s[52:55], s67 offen offset:1024 nt lds\n"
    "v_mfma_f32_16x16x32_bf16 v[104:107], a[200:203], a[164:167], v[104:107]\n"
    "v_add_f32 v219, v219, v185\n"
    "ds_read_b128 a[196:199], v188 offset:8448\n"
    "v_mfma_f32_16x16x32_bf16 v[108:111], a[204:207], a[100:103], v[108:111]\n"
    "v_exp_f32 v187, v139\n"
    "v_mfma_f32_16x16x32_bf16 v[112:115], a[204:207], a[132:135], v[112:115]\n"
    "v_cvt_pk_bf16_f32 v163, v186, v187\n"
    "v_add_f32 v219, v219, v186\n"
    "v_mfma_f32_16x16x32_bf16 v[116:119], a[204:207], a[164:167], v[116:119]\n"
    "s_add_u32 m0, s91, 73728\n"
    "s_waitcnt lgkmcnt(2)\n"
    "v_mfma_f32_16x16x32_bf16 v[96:99], a[208:211], a[104:107], v[96:99]\n"
    "ds_write_b128 v172, v[160:163] offset:13312\n"
    "v_add_f32 v219, v219, v187\n"
    "v_mfma_f32_16x16x32_bf16 v[100:103], a[208:211], a[136:139], v[100:103]\n"
    "buffer_load_dwordx4 v210, s[52:55], s67 offen offset:0 nt lds\n"
    "ds_read_b128 a[200:203], v189 offset:256\n"
    "v_mfma_f32_16x16x32_bf16 v[104:107], a[208:211], a[168:171], v[104:107]\n"
    "v_exp_f32 v180, v128\n"
    "v_mfma_f32_16x16x32_bf16 v[108:111], a[212:215], a[104:107], v[108:111]\n"
    "ds_read_b128 a[204:207], v189 offset:8448\n"
    "ds_read_b128 a[208:211], v190 offset:256\n"
    "v_mfma_f32_16x16x32_bf16 v[112:115], a[212:215], a[136:139], v[112:115]\n"
    "v_exp_f32 v181, v129\n"
    "v_mfma_f32_16x16x32_bf16 v[116:119], a[212:215], a[168:171], v[116:119]\n"
    "v_exp_f32 v182, v130\n"
    "v_cvt_pk_bf16_f32 v164, v180, v181\n"
    "v_mfma_f32_16x16x32_bf16 v[96:99], a[216:219], a[108:111], v[96:99]\n"
    "v_add_f32 v220, v180, v181\n"
    "ds_read_b128 a[212:215], v190 offset:8448\n"
    "v_mfma_f32_16x16x32_bf16 v[100:103], a[216:219], a[140:143], v[100:103]\n"
    "buffer_load_dwordx4 v211, s[52:55], s67 offen offset:1024 nt lds\n"
    "v_mfma_f32_16x16x32_bf16 v[104:107], a[216:219], a[172:175], v[104:107]\n"
    "v_exp_f32 v183, v131\n"
    "s_nop 0\n"
    "v_cvt_pk_bf16_f32 v165, v182, v183\n"
    "v_mfma_f32_16x16x32_bf16 v[108:111], a[220:223], a[108:111], v[108:111]\n"
    "v_add_f32 v220, v220, v182\n"
    "v_add_f32 v220, v220, v183\n"
    "v_mfma_f32_16x16x32_bf16 v[112:115], a[220:223], a[140:143], v[112:115]\n"
    "s_add_u32 m0, s91, 81920\n"
    "ds_read_b128 a[216:219], v191 offset:256\n"
    "v_mfma_f32_16x16x32_bf16 v[116:119], a[220:223], a[172:175], v[116:119]\n"
    "s_waitcnt lgkmcnt(3)\n"
    "v_mfma_f32_16x16x32_bf16 v[96:99], a[192:195], a[112:115], v[96:99]\n"
    "v_exp_f32 v184, v140\n"
    "v_mfma_f32_16x16x32_bf16 v[100:103], a[192:195], a[144:147], v[100:103]\n"
    "buffer_load_dwordx4 v208, s[52:55], s68 offen offset:0 nt lds\n"
    "ds_read_b128 a[220:223], v191 offset:8448\n"
    "v_mfma_f32_16x16x32_bf16 v[104:107], a[192:195], a[176:179], v[104:107]\n"
    "v_exp_f32 v185, v141\n"
    "v_mfma_f32_16x16x32_bf16 v[108:111], a[196:199], a[112:115], v[108:111]\n"
    "v_exp_f32 v186, v142\n"
    "v_cvt_pk_bf16_f32 v166, v184, v185\n"
    "v_add_f32 v220, v220, v184\n"
    "v_mfma_f32_16x16x32_bf16 v[112:115], a[196:199], a[144:147], v[112:115]\n"
    "v_add_f32 v220, v220, v185\n"
    "v_mfma_f32_16x16x32_bf16 v[116:119], a[196:199], a[176:179], v[116:119]\n"
    "v_exp_f32 v187, v143\n"
    "s_nop 0\n"
    "v_cvt_pk_bf16_f32 v167, v186, v187\n"
    "v_mfma_f32_16x16x32_bf16 v[96:99], a[200:203], a[116:119], v[96:99]\n"
    "v_add_f32 v220, v220, v186\n"
    "ds_write_b128 v172, v[164:167] offset:14336\n"
    "v_mfma_f32_16x16x32_bf16 v[100:103], a[200:203], a[148:151], v[100:103]\n"
    "buffer_load_dwordx4 v209, s[52:55], s68 offen offset:1024 nt lds\n"
    "v_add_f32 v220, v220, v187\n"
    "v_mfma_f32_16x16x32_bf16 v[104:107], a[200:203], a[180:183], v[104:107]\n"
    "v_max3_f32 v221, v218, v219, v220\n"
    "v_mfma_f32_16x16x32_bf16 v[108:111], a[204:207], a[116:119], v[108:111]\n"
    "v_cmp_lt_f32 vcc, s70, v221\n"
    "s_cbranch_vccnz .Lfw0_%=\n"
    ".Lfb0_%=:\n"
    "v_mfma_f32_16x16x32_bf16 v[112:115], a[204:207], a[148:151], v[112:115]\n"
    "s_add_u32 m0, s91, 90112\n"
    "v_mfma_f32_16x16x32_bf16 v[116:119], a[204:207], a[180:183], v[116:119]\n"
    "s_waitcnt lgkmcnt(1)\n"
    "v_mfma_f32_16x16x32_bf16 v[96:99], a[208:211], a[120:123], v[96:99]\n"
    "v_mfma_f32_16x16x32_bf16 v[100:103], a[208:211], a[152:155], v[100:103]\n"
    "buffer_load_dwordx4 v210, s[52:55], s68 offen offset:0 nt lds\n"
    "v_mfma_f32_16x16x32_bf16 v[104:107], a[208:211], a[184:187], v[104:107]\n"
    "v_mfma_f32_16x16x32_bf16 v[108:111], a[212:215], a[120:123], v[108:111]\n"
    "v_mfma_f32_16x16x32_bf16 v[112:115], a[212:215], a[152:155], v[112:115]\n"
    "s_waitcnt vmcnt(15) lgkmcnt(0)\n"
    "s_barrier\n"
    "v_mfma_f32_16x16x32_bf16 v[116:119], a[212:215], a[184:187], v[116:119]\n"
    "ds_read_b32 v168, v175 offset:0\n"
    "ds_read_b128 a[0:3], v173 offset:12288\n"
    "v_mfma_f32_16x16x32_bf16 v[96:99], a[216:219], a[124:127], v[96:99]\n"
    "ds_read_b128 a[4:7], v173 offset:13312\n"
    "ds_read_b128 a[8:11], v173 offset:14336\n"
    "v_mfma_f32_16x16x32_bf16 v[100:103], a[216:219], a[156:159], v[100:103]\n"
    "ds_read_b64_tr_b16 a[224:225], v192 offset:32768\n"
    "ds_read_b64_tr_b16 a[226:227], v192 offset:40960\n"
    "v_mfma_f32_16x16x32_bf16 v[104:107], a[216:219], a[188:191], v[104:107]\n"
    "ds_read_b64_tr_b16 a[228:229], v193 offset:32768\n"
    "ds_read_b64_tr_b16 a[230:231], v193 offset:40960\n"
    "buffer_load_dwordx4 v211, s[52:55], s68 offen offset:1024 nt lds\n"
    "v_mfma_f32_16x16x32_bf16 v[108:111], a[220:223], a[124:127], v[108:111]\n"
    "ds_read_b64_tr_b16 a[232:233], v194 offset:32768\n"
    "ds_read_b64_tr_b16 a[234:235], v194 offset:40960\n"
    "s_waitcnt lgkmcnt(9)\n"
    "v_cmp_ne_u32 vcc, 0, v168\n"
    "s_cbranch_vccnz .Ltr00_%=\n"
    ".Lbk00_%=:\n"
    "v_mfma_f32_16x16x32_bf16 v[112:115], a[220:223], a[156:159], v[112:115]\n"
    "ds_read_b64_tr_b16 a[236:237], v195 offset:32768\n"
    "ds_read_b64_tr_b16 a[238:239], v195 offset:40960\n"
    "s_add_u32 s67, s67, 32768\n"
    "s_add_u32 s68, s68, 32768\n"
    "v_mfma_f32_16x16x32_bf16 v[116:119], a[220:223], a[188:191], v[116:119]\n"
    "s_waitcnt lgkmcnt(4)\n"
    "v_mfma_f32_16x16x32_bf16 v[0:3], a[224:227], a[0:3], v[0:3]\n"
    "s_add_u32 m0, s91, 0\n"
    "ds_read_b64_tr_b16 a[240:241], v196 offset:32768\n"
    "v_mfma_f32_16x16x32_bf16 v[4:7], a[224:227], a[4:7], v[4:7]\n"
    "ds_read_b64_tr_b16 a[242:243], v196 offset:40960\n"
    "buffer_load_dwordx4 v200, s[48:51], s66 offen offset:0 nt lds\n"
    "v_mfma_f32_16x16x32_bf16 v[8:11], a[224:227], a[8:11], v[8:11]\n"
    "ds_read_b64_tr_b16 a[244:245], v197 offset:32768\n"
    "ds_read_b64_tr_b16 a[246:247], v197 offset:40960\n"
    "v_mfma_f32_16x16x32_bf16 v[12:15], a[228:231], a[0:3], v[12:15]\n"
    "ds_read_b64_tr_b16 a[248:249], v198 offset:32768\n"
    "ds_read_b64_tr_b16 a[250:251], v198 offset:40960\n"
    "v_mfma_f32_16x16x32_bf16 v[16:19], a[228:231], a[4:7], v[16:19]\n"
    "v_exp_f32 v180, v96\n"
    "v_mfma_f32_16x16x32_bf16 v[20:23], a[228:231], a[8:11], v[20:23]\n"
    "ds_read_b128 a[12:15], v173 offset:18432\n"
    "ds_read_b128 a[16:19], v173 offset:19456\n"
    "ds_read_b128 a[20:23], v173 offset:20480\n"
    "ds_read_b64_tr_b16 a[252:253], v199 offset:32768\n"
    "ds_read_b64_tr_b16 a[254:255], v199 offset:40960\n"
    "s_waitcnt lgkmcnt(11)\n"
    "v_mfma_f32_16x16x32_bf16 v[24:27], a[232:235], a[0:3], v[24:27]\n"
    "v_add_f32 v212, v212, v218\n"
    "v_add_f32 v213, v213, v219\n"
    "v_mfma_f32_16x16x32_bf16 v[28:31], a[232:235], a[4:7], v[28:31]\n"
    "buffer_load_dwordx4 v201, s[48:51], s66 offen offset:1024 nt lds\n"
    "v_add_f32 v214, v214, v220\n"
    "v_mfma_f32_16x16x32_bf16 v[32:35], a[232:235], a[8:11], v[32:35]\n"
    "v_exp_f32 v181, v97\n"
    "v_mfma_f32_16x16x32_bf16 v[36:39], a[236:239], a[0:3], v[36:39]\n"
    "v_exp_f32 v182, v98\n"
    "v_mfma_f32_16x16x32_bf16 v[40:43], a[236:239], a[4:7], v[40:43]\n"
    "s_add_u32 m0, s91, 8192\n"
    "v_cvt_pk_bf16_f32 v156, v180, v181\n"
    "v_mfma_f32_16x16x32_bf16 v[44:47], a[236:239], a[8:11], v[44:47]\n"
    "ds_read_b64_tr_b16 a[224:225], v192 offset:49152\n"
    "s_waitcnt lgkmcnt(1)\n"
    "v_mfma_f32_16x16x32_bf16 v[48:51], a[240:243], a[0:3], v[48:51]\n"
    "ds_read_b64_tr_b16 a[226:227], v192 offset:57344\n"
    "ds_read_b64_tr_b16 a[228:229], v193 offset:49152\n"
    "v_mfma_f32_16x16x32_bf16 v[52:55], a[240:243], a[4:7], v[52:55]\n"
    "buffer_load_dwordx4 v202, s[48:51], s66 offen offset:0 nt lds\n"
    "ds_read_b64_tr_b16 a[230:231], v193 offset:57344\n"
    "v_mfma_f32_16x16x32_bf16 v[56:59], a[240:243], a[8:11], v[56:59]\n"
    "v_exp_f32 v183, v99\n"
    "v_mfma_f32_16x16x32_bf16 v[60:63], a[244:247], a[0:3], v[60:63]\n"
    "v_add_f32 v215, v180, v181\n"
    "ds_read_b64_tr_b16 a[232:233], v194 offset:49152\n"
    "v_mfma_f32_16x16x32_bf16 v[64:67], a[244:247], a[4:7], v[64:67]\n"
    "ds_read_b64_tr_b16 a[234:235], v194 offset:57344\n"
    "ds_read_b64_tr_b16 a[236:237], v195 offset:49152\n"
    "v_mfma_f32_16x16x32_bf16 v[68:71], a[244:247], a[8:11], v[68:71]\n"
    "ds_read_b64_tr_b16 a[238:239], v195 offset:57344\n"
    "v_cvt_pk_bf16_f32 v157, v182, v183\n"
    "v_mfma_f32_16x16x32_bf16 v[72:75], a[248:251], a[0:3], v[72:75]\n"
    "v_exp_f32 v184, v108\n"
    "v_mfma_f32_16x16x32_bf16 v[76:79], a[248:251], a[4:7], v[76:79]\n"
    "v_add_f32 v215, v215, v182\n"
    "buffer_load_dwordx4 v203, s[48:51], s66 offen offset:1024 nt lds\n"
    "v_mfma_f32_16x16x32_bf16 v[80:83], a[248:251], a[8:11], v[80:83]\n"
    "v_add_f32 v215, v215, v183\n"
    "ds_read_b64_tr_b16 a[240:241], v196 offset:49152\n"
    "v_mfma_f32_16x16x32_bf16 v[84:87], a[252:255], a[0:3], v[84:87]\n"
    "v_exp_f32 v185, v109\n"
    "v_mfma_f32_16x16x32_bf16 v[88:91], a[252:255], a[4:7], v[88:91]\n"
    "s_add_u32 m0, s91, 16384\n"
    "ds_read_b64_tr_b16 a[242:243], v196 offset:57344\n"
    "v_mfma_f32_16x16x32_bf16 v[92:95], a[252:255], a[8:11], v[92:95]\n"
    "ds_read_b64_tr_b16 a[244:245], v197 offset:49152\n"
    "s_waitcnt lgkmcnt(3)\n"
    "v_mfma_f32_16x16x32_bf16 v[0:3], a[224:227], a[12:15], v[0:3]\n"
    "v_exp_f32 v186, v110\n"
    "v_cvt_pk_bf16_f32 v158, v184, v185\n"
    "v_mfma_f32_16x16x32_bf16 v[4:7], a[224:227], a[16:19], v[4:7]\n"
    "buffer_load_dwordx4 v204, s[48:51], s66 offen offset:0 nt lds\n"
    "v_add_f32 v215, v215, v184\n"
    "v_mfma_f32_16x16x32_bf16 v[8:11], a[224:227], a[20:23], v[8:11]\n"
    "ds_read_b64_tr_b16 a[246:247], v197 offset:57344\n"
    "ds_read_b64_tr_b16 a[248:249], v198 offset:49152\n"
    "v_mfma_f32_16x16x32_bf16 v[12:15], a[228:231], a[12:15], v[12:15]\n"
    "ds_read_b64_tr_b16 a[250:251], v198 offset:57344\n"
    "v_add_f32 v215, v215, v185\n"
    "v_mfma_f32_16x16x32_bf16 v[16:19], a[228:231], a[16:19], v[16:19]\n"
    "ds_read_b64_tr_b16 a[252:253], v199 offset:49152\n"
    "ds_read_b64_tr_b16 a[254:255], v199 offset:57344\n"
    "v_mfma_f32_16x16x32_bf16 v[20:23], a[228:231], a[20:23], v[20:23]\n"
    "v_exp_f32 v187, v111\n"
    "v_mfma_f32_16x16x32_bf16 v[24:27], a[232:235], a[12:15], v[24:27]\n"
    "v_cvt_pk_bf16_f32 v159, v186, v187\n"
    "v_add_f32 v215, v215, v186\n"
    "v_mfma_f32_16x16x32_bf16 v[28:31], a[232:235], a[16:19], v[28:31]\n"
    "buffer_load_dwordx4 v205, s[48:51], s66 offen offset:1024 nt lds\n"
    "ds_write_b128 v172, v[156:159] offset:0\n"
    "v_mfma_f32_16x16x32_bf16 v[32:35], a[232:235], a[20:23], v[32:35]\n"
    "v_exp_f32 v180, v100\n"
    "v_mfma_f32_16x16x32_bf16 v[36:39], a[236:239], a[12:15], v[36:39]\n"
    "v_exp_f32 v181, v101\n"
    "v_mfma_f32_16x16x32_bf16 v[40:43], a[236:239], a[16:19], v[40:43]\n"
    "v_add_f32 v215, v215, v187\n"
    "s_add_u32 m0, s91, 24576\n"
    "v_mfma_f32_16x16x32_bf16 v[44:47], a[236:239], a[20:23], v[44:47]\n"
    "s_waitcnt lgkmcnt(1)\n"
    "v_mfma_f32_16x16x32_bf16 v[48:51], a[240:243], a[12:15], v[48:51]\n"
    "v_exp_f32 v182, v102\n"
    "v_mfma_f32_16x16x32_bf16 v[52:55], a[240:243], a[16:19], v[52:55]\n"
    "buffer_load_dwordx4 v206, s[48:51], s66 offen offset:0 nt lds\n"
    "v_cvt_pk_bf16_f32 v160, v180, v181\n"
    "v_mfma_f32_16x16x32_bf16 v[56:59], a[240:243], a[20:23], v[56:59]\n"
    "v_add_f32 v216, v180, v181\n"
    "v_mfma_f32_16x16x32_bf16 v[60:63], a[244:247], a[12:15], v[60:63]\n"
    "v_exp_f32 v183, v103\n"
    "v_mfma_f32_16x16x32_bf16 v[64:67], a[244:247], a[16:19], v[64:67]\n"
    "s_waitcnt vmcnt(15) lgkmcnt(0)\n"
    "s_barrier\n"
    "v_mfma_f32_16x16x32_bf16 v[68:71], a[244:247], a[20:23], v[68:71]\n"
    "ds_read_b128 a[192:195], v188 offset:32768\n"
    "ds_read_b128 a[196:199], v188 offset:40960\n"
    "v_mfma_f32_16x16x32_bf16 v[72:75], a[248:251], a[12:15], v[72:75]\n"
    "v_cvt_pk_bf16_f32 v161, v182, v183\n"
    "ds_read_b128 a[200:203], v189 offset:32768\n"
    "v_mfma_f32_16x16x32_bf16 v[76:79], a[248:251], a[16:19], v[76:79]\n"
    "ds_read_b128 a[204:207], v189 offset:40960\n"
    "buffer_load_dwordx4 v207, s[48:51], s66 offen offset:1024 nt lds\n"
    "v_mfma_f32_16x16x32_bf16 v[80:83], a[248:251], a[20:23], v[80:83]\n"
    "v_add_f32 v216, v216, v182\n"
    "s_add_u32 s66, s66, 32768\n"
    "v_mfma_f32_16x16x32_bf16 v[84:87], a[252:255], a[12:15], v[84:87]\n"
    "v_add_f32 v216, v216, v183\n"
    "v_mfma_f32_16x16x32_bf16 v[88:91], a[252:255], a[16:19], v[88:91]\n"
    "v_mfma_f32_16x16x32_bf16 v[92:95], a[252:255], a[20:23], v[92:95]\n"
    "s_waitcnt lgkmcnt(2)\n"
    "v_mfma_f32_16x16x32_bf16 v[120:123], a[192:195], a[96:99], v[144:147]\n"
    "s_add_u32 m0, s91, 98304\n"
    "ds_read_b128 a[208:211], v190 offset:32768\n"
    "v_mfma_f32_16x16x32_bf16 v[124:127], a[192:195], a[128:131], v[148:151]\n"
    "buffer_load_dwordx4 v208, s[52:55], s67 offen offset:0 nt lds\n"
    "ds_read_b128 a[212:215], v190 offset:40960\n"
    "v_mfma_f32_16x16x32_bf16 v[128:131], a[192:195], a[160:163], v[152:155]\n"
    "v_exp_f32 v184, v112\n"
    "v_mfma_f32_16x16x32_bf16 v[132:135], a[196:199], a[96:99], v[144:147]\n"
    "ds_read_b128 a[216:219], v191 offset:32768\n"
    "ds_read_b128 a[220:223], v191 offset:40960\n"
    "v_mfma_f32_16x16x32_bf16 v[136:139], a[196:199], a[128:131], v[148:151]\n"
    "v_exp_f32 v185, v113\n"
    "v_mfma_f32_16x16x32_bf16 v[140:143], a[196:199], a[160:163], v[152:155]\n"
    "ds_read_b128 a[192:195], v188 offset:33024\n"
    "s_waitcnt lgkmcnt(5)\n"
    "v_mfma_f32_16x16x32_bf16 v[120:123], a[200:203], a[100:103], v[120:123]\n"
    "v_exp_f32 v186, v114\n"
    "v_cvt_pk_bf16_f32 v162, v184, v185\n"
    "v_mfma_f32_16x16x32_bf16 v[124:127], a[200:203], a[132:135], v[124:127]\n"
    "v_add_f32 v216, v216, v184\n"
    "buffer_load_dwordx4 v209, s[52:55], s67 offen offset:1024 nt lds\n"
    "v_mfma_f32_16x16x32_bf16 v[128:131], a[200:203], a[164:167], v[128:131]\n"
    "v_add_f32 v216, v216, v185\n"
    "ds_read_b128 a[196:199], v188 offset:41216\n"
    "v_mfma_f32_16x16x32_bf16 v[132:135], a[204:207], a[100:103], v[132:135]\n"
    "v_exp_f32 v187, v115\n"
    "v_mfma_f32_16x16x32_bf16 v[136:139], a[204:207], a[132:135], v[136:139]\n"
    "v_cvt_pk_bf16_f32 v163, v186, v187\n"
    "v_add_f32 v216, v216, v186\n"
    "v_mfma_f32_16x16x32_bf16 v[140:143], a[204:207], a[164:167], v[140:143]\n"
    "s_add_u32 m0, s91, 106496\n"
    "s_waitcnt lgkmcnt(2)\n"
    "v_mfma_f32_16x16x32_bf16 v[120:123], a[208:211], a[104:107], v[120:123]\n"
    "ds_write_b128 v172, v[160:163] offset:1024\n"
    "v_add_f32 v216, v216, v187\n"
    "v_mfma_f32_16x16x32_bf16 v[124:127], a[208:211], a[136:139], v[124:127]\n"
    "buffer_load_dwordx4 v210, s[52:55], s67 offen offset:0 nt lds\n"
    "ds_read_b128 a[200:203], v189 offset:33024\n"
    "v_mfma_f32_16x16x32_bf16 v[128:131], a[208:211], a[168:171], v[128:131]\n"
    "v_exp_f32 v180, v104\n"
    "v_mfma_f32_16x16x32_bf16 v[132:135], a[212:215], a[104:107], v[132:135]\n"
    "ds_read_b128 a[204:207], v189 offset:41216\n"
    "ds_read_b128 a[208:211], v190 offset:33024\n"
    "v_mfma_f32_16x16x32_bf16 v[136:139], a[212:215], a[136:139], v[136:139]\n"
    "v_exp_f32 v181, v105\n"
    "v_mfma_f32_16x16x32_bf16 v[140:143], a[212:215], a[168:171], v[140:143]\n"
    "v_exp_f32 v182, v106\n"
    "v_cvt_pk_bf16_f32 v164, v180, v181\n"
    "v_mfma_f32_16x16x32_bf16 v[120:123], a[216:219], a[108:111], v[120:123]\n"
    "v_add_f32 v217, v180, v181\n"
    "ds_read_b128 a[212:215], v190 offset:41216\n"
    "v_mfma_f32_16x16x32_bf16 v[124:127], a[216:219], a[140:143], v[124:127]\n"
    "buffer_load_dwordx4 v211, s[52:55], s67 offen offset:1024 nt lds\n"
    "v_mfma_f32_16x16x32_bf16 v[128:131], a[216:219], a[172:175], v[128:131]\n"
    "v_exp_f32 v183, v107\n"
    "s_nop 0\n"
    "v_cvt_pk_bf16_f32 v165, v182, v183\n"
    "v_mfma_f32_16x16x32_bf16 v[132:135], a[220:223], a[108:111], v[132:135]\n"
    "v_add_f32 v217, v217, v182\n"
    "v_add_f32 v217, v217, v183\n"
    "v_mfma_f32_16x16x32_bf16 v[136:139], a[220:223], a[140:143], v[136:139]\n"
    "s_add_u32 m0, s91, 114688\n"
    "ds_read_b128 a[216:219], v191 offset:33024\n"
    "v_mfma_f32_16x16x32_bf16 v[140:143], a[220:223], a[172:175], v[140:143]\n"
    "s_waitcnt lgkmcnt(3)\n"
    "v_mfma_f32_16x16x32_bf16 v[120:123], a[192:195], a[112:115], v[120:123]\n"
    "v_exp_f32 v184, v116\n"
    "v_mfma_f32_16x16x32_bf16 v[124:127], a[192:195], a[144:147], v[124:127]\n"
    "buffer_load_dwordx4 v208, s[52:55], s68 offen offset:0 nt lds\n"
    "ds_read_b128 a[220:223], v191 offset:41216\n"
    "v_mfma_f32_16x16x32_bf16 v[128:131], a[192:195], a[176:179], v[128:131]\n"
    "v_exp_f32 v185, v117\n"
    "v_mfma_f32_16x16x32_bf16 v[132:135], a[196:199], a[112:115], v[132:135]\n"
    "v_exp_f32 v186, v118\n"
    "v_cvt_pk_bf16_f32 v166, v184, v185\n"
    "v_add_f32 v217, v217, v184\n"
    "v_mfma_f32_16x16x32_bf16 v[136:139], a[196:199], a[144:147], v[136:139]\n"
    "v_add_f32 v217, v217, v185\n"
    "v_mfma_f32_16x16x32_bf16 v[140:143], a[196:199], a[176:179], v[140:143]\n"
    "v_exp_f32 v187, v119\n"
    "s_nop 0\n"
    "v_cvt_pk_bf16_f32 v167, v186, v187\n"
    "v_mfma_f32_16x16x32_bf16 v[120:123], a[200:203], a[116:119], v[120:123]\n"
    "v_add_f32 v217, v217, v186\n"
    "ds_write_b128 v172, v[164:167] offset:2048\n"
    "v_mfma_f32_16x16x32_bf16 v[124:127], a[200:203], a[148:151], v[124:127]\n"
    "buffer_load_dwordx4 v209, s[52:55], s68 offen offset:1024 nt lds\n"
    "v_add_f32 v217, v217, v187\n"
    "v_mfma_f32_16x16x32_bf16 v[128:131], a[200:203], a[180:183], v[128:131]\n"
    "v_max3_f32 v221, v215, v216, v217\n"
    "v_mfma_f32_16x16x32_bf16 v[132:135], a[204:207], a[116:119], v[132:135]\n"
    "v_cmp_lt_f32 vcc, s70, v221\n"
    "s_cbranch_vccnz .Lfw1_%=\n"
    ".Lfb1_%=:\n"
    "v_mfma_f32_16x16x32_bf16 v[136:139], a[204:207], a[148:151], v[136:139]\n"
    "s_add_u32 m0, s91, 122880\n"
    "v_mfma_f32_16x16x32_bf16 v[140:143], a[204:207], a[180:183], v[140:143]\n"
    "s_waitcnt lgkmcnt(1)\n"
    "v_mfma_f32_16x16x32_bf16 v[120:123], a[208:211], a[120:123], v[120:123]\n"
    "v_mfma_f32_16x16x32_bf16 v[124:127], a[208:211], a[152:155], v[124:127]\n"
    "buffer_load_dwordx4 v210, s[52:55], s68 offen offset:0 nt lds\n"
    "v_mfma_f32_16x16x32_bf16 v[128:131], a[208:211], a[184:187], v[128:131]\n"
    "v_mfma_f32_16x16x32_bf16 v[132:135], a[212:215], a[120:123], v[132:135]\n"
    "v_mfma_f32_16x16x32_bf16 v[136:139], a[212:215], a[152:155], v[136:139]\n"
    "s_waitcnt vmcnt(15) lgkmcnt(0)\n"
    "s_barrier\n"
    "v_mfma_f32_16x16x32_bf16 v[140:143], a[212:215], a[184:187], v[140:143]\n"
    "ds_read_b32 v168, v175 offset:0\n"
    "ds_read_b128 a[0:3], v173 offset:0\n"
    "v_mfma_f32_16x16x32_bf16 v[120:123], a[216:219], a[124:127], v[120:123]\n"
    "ds_read_b128 a[4:7], v173 offset:1024\n"
    "ds_read_b128 a[8:11], v173 offset:2048\n"
    "v_mfma_f32_16x16x32_bf16 v[124:127], a[216:219], a[156:159], v[124:127]\n"
    "ds_read_b64_tr_b16 a[224:225], v192 offset:0\n"
    "ds_read_b64_tr_b16 a[226:227], v192 offset:8192\n"
    "v_mfma_f32_16x16x32_bf16 v[128:131], a[216:219], a[188:191], v[128:131]\n"
    "ds_read_b64_tr_b16 a[228:229], v193 offset:0\n"
    "ds_read_b64_tr_b16 a[230:231], v193 offset:8192\n"
    "buffer_load_dwordx4 v211, s[52:55], s68 offen offset:1024 nt lds\n"
    "v_mfma_f32_16x16x32_bf16 v[132:135], a[220:223], a[124:127], v[132:135]\n"
    "ds_read_b64_tr_b16 a[232:233], v194 offset:0\n"
    "ds_read_b64_tr_b16 a[234:235], v194 offset:8192\n"
    "s_waitcnt lgkmcnt(9)\n"
    "v_cmp_ne_u32 vcc, 0, v168\n"
    "s_cbranch_vccnz .Ltr01_%=\n"
    ".Lbk01_%=:\n"
    "v_mfma_f32_16x16x32_bf16 v[136:139], a[220:223], a[156:159], v[136:139]\n"
    "ds_read_b64_tr_b16 a[236:237], v195 offset:0\n"
    "ds_read_b64_tr_b16 a[238:239], v195 offset:8192\n"
    "s_add_u32 s67, s67, 32768\n"
    "s_add_u32 s68, s68, 32768\n"
    "v_mfma_f32_16x16x32_bf16 v[140:143], a[220:223], a[188:191], v[140:143]\n"
    "s_waitcnt lgkmcnt(4)\n"
    "v_mfma_f32_16x16x32_bf16 v[0:3], a[224:227], a[0:3], v[0:3]\n"
    "s_add_u32 m0, s91, 32768\n"
    "ds_read_b64_tr_b16 a[240:241], v196 offset:0\n"
    "v_mfma_f32_16x16x32_bf16 v[4:7], a[224:227], a[4:7], v[4:7]\n"
    "ds_read_b64_tr_b16 a[242:243], v196 offset:8192\n"
    "buffer_load_dwordx4 v200, s[48:51], s66 offen offset:0 nt lds\n"
    "v_mfma_f32_16x16x32_bf16 v[8:11], a[224:227], a[8:11], v[8:11]\n"
    "ds_read_b64_tr_b16 a[244:245], v197 offset:0\n"
    "ds_read_b64_tr_b16 a[246:247], v197 offset:8192\n"
    "v_mfma_f32_16x16x32_bf16 v[12:15], a[228:231], a[0:3], v[12:15]\n"
    "ds_read_b64_tr_b16 a[248:249], v198 offset:0\n"
    "ds_read_b64_tr_b16 a[250:251], v198 offset:8192\n"
    "v_mfma_f32_16x16x32_bf16 v[16:19], a[228:231], a[4:7], v[16:19]\n"
    "v_exp_f32 v180, v120\n"
    "v_mfma_f32_16x16x32_bf16 v[20:23], a[228:231], a[8:11], v[20:23]\n"
    "ds_read_b128 a[12:15], v173 offset:6144\n"
    "ds_read_b128 a[16:19], v173 offset:7168\n"
    "ds_read_b128 a[20:23], v173 offset:8192\n"
    "ds_read_b64_tr_b16 a[252:253], v199 offset:0\n"
    "ds_read_b64_tr_b16 a[254:255], v199 offset:8192\n"
    "s_waitcnt lgkmcnt(11)\n"
    "v_mfma_f32_16x16x32_bf16 v[24:27], a[232:235], a[0:3], v[24:27]\n"
    "v_add_f32 v212, v212, v215\n"
    "v_add_f32 v213, v213, v216\n"
    "v_mfma_f32_16x16x32_bf16 v[28:31], a[232:235], a[4:7], v[28:31]\n"
    "buffer_load_dwordx4 v201, s[48:51], s66 offen offset:1024 nt lds\n"
    "v_add_f32 v214, v214, v217\n"
    "v_mfma_f32_16x16x32_bf16 v[32:35], a[232:235], a[8:11], v[32:35]\n"
    "v_exp_f32 v181, v121\n"
    "v_mfma_f32_16x16x32_bf16 v[36:39], a[236:239], a[0:3], v[36:39]\n"
    "v_exp_f32 v182, v122\n"
    "v_mfma_f32_16x16x32_bf16 v[40:43], a[236:239], a[4:7], v[40:43]\n"
    "s_add_u32 m0, s91, 40960\n"
    "v_cvt_pk_bf16_f32 v156, v180, v181\n"
    "v_mfma_f32_16x16x32_bf16 v[44:47], a[236:239], a[8:11], v[44:47]\n"
    "ds_read_b64_tr_b16 a[224:225], v192 offset:16384\n"
    "s_waitcnt lgkmcnt(1)\n"
    "v_mfma_f32_16x16x32_bf16 v[48:51], a[240:243], a[0:3], v[48:51]\n"
    "ds_read_b64_tr_b16 a[226:227], v192 offset:24576\n"
    "ds_read_b64_tr_b16 a[228:229], v193 offset:16384\n"
    "v_mfma_f32_16x16x32_bf16 v[52:55], a[240:243], a[4:7], v[52:55]\n"
    "buffer_load_dwordx4 v202, s[48:51], s66 offen offset:0 nt lds\n"
    "ds_read_b64_tr_b16 a[230:231], v193 offset:24576\n"
    "v_mfma_f32_16x16x32_bf16 v[56:59], a[240:243], a[8:11], v[56:59]\n"
    "v_exp_f32 v183, v123\n"
    "v_mfma_f32_16x16x32_bf16 v[60:63], a[244:247], a[0:3], v[60:63]\n"
    "v_add_f32 v218, v180, v181\n"
    "ds_read_b64_tr_b16 a[232:233], v194 offset:16384\n"
    "v_mfma_f32_16x16x32_bf16 v[64:67], a[244:247], a[4:7], v[64:67]\n"
    "ds_read_b64_tr_b16 a[234:235], v194 offset:24576\n"
    "ds_read_b64_tr_b16 a[236:237], v195 offset:16384\n"
    "v_mfma_f32_16x16x32_bf16 v[68:71], a[244:247], a[8:11], v[68:71]\n"
    "ds_read_b64_tr_b16 a[238:239], v195 offset:24576\n"
    "v_cvt_pk_bf16_f32 v157, v182, v183\n"
    "v_mfma_f32_16x16x32_bf16 v[72:75], a[248:251], a[0:3], v[72:75]\n"
    "v_exp_f32 v184, v132\n"
    "v_mfma_f32_16x16x32_bf16 v[76:79], a[248:251], a[4:7], v[76:79]\n"
    "v_add_f32 v218, v218, v182\n"
    "buffer_load_dwordx4 v203, s[48:51], s66 offen offset:1024 nt lds\n"
    "v_mfma_f32_16x16x32_bf16 v[80:83], a[248:251], a[8:11], v[80:83]\n"
    "v_add_f32 v218, v218, v183\n"
    "ds_read_b64_tr_b16 a[240:241], v196 offset:16384\n"
    "v_mfma_f32_16x16x32_bf16 v[84:87], a[252:255], a[0:3], v[84:87]\n"
    "v_exp_f32 v185, v133\n"
    "v_mfma_f32_16x16x32_bf16 v[88:91], a[252:255], a[4:7], v[88:91]\n"
    "s_add_u32 m0, s91, 49152\n"
    "ds_read_b64_tr_b16 a[242:243], v196 offset:24576\n"
    "v_mfma_f32_16x16x32_bf16 v[92:95], a[252:255], a[8:11], v[92:95]\n"
    "ds_read_b64_tr_b16 a[244:245], v197 offset:16384\n"
    "s_waitcnt lgkmcnt(3)\n"
    "v_mfma_f32_16x16x32_bf16 v[0:3], a[224:227], a[12:15], v[0:3]\n"
    "v_exp_f32 v186, v134\n"
    "v_cvt_pk_bf16_f32 v158, v184, v185\n"
    "v_mfma_f32_16x16x32_bf16 v[4:7], a[224:227], a[16:19], v[4:7]\n"
    "buffer_load_dwordx4 v204, s[48:51], s66 offen offset:0 nt lds\n"
    "v_add_f32 v218, v218, v184\n"
    "v_mfma_f32_16x16x32_bf16 v[8:11], a[224:227], a[20:23], v[8:11]\n"
    "ds_read_b64_tr_b16 a[246:247], v197 offset:24576\n"
    "ds_read_b64_tr_b16 a[248:249], v198 offset:16384\n"
    "v_mfma_f32_16x16x32_bf16 v[12:15], a[228:231], a[12:15], v[12:15]\n"
    "ds_read_b64_tr_b16 a[250:251], v198 offset:24576\n"
    "v_add_f32 v218, v218, v185\n"
    "v_mfma_f32_16x16x32_bf16 v[16:19], a[228:231], a[16:19], v[16:19]\n"
    "ds_read_b64_tr_b16 a[252:253], v199 offset:16384\n"
    "ds_read_b64_tr_b16 a[254:255], v199 offset:24576\n"
    "v_mfma_f32_16x16x32_bf16 v[20:23], a[228:231], a[20:23], v[20:23]\n"
    "v_exp_f32 v187, v135\n"
    "v_mfma_f32_16x16x32_bf16 v[24:27], a[232:235], a[12:15], v[24:27]\n"
    "v_cvt_pk_bf16_f32 v159, v186, v187\n"
    "v_add_f32 v218, v218, v186\n"
    "v_mfma_f32_16x16x32_bf16 v[28:31], a[232:235], a[16:19], v[28:31]\n"
    "buffer_load_dwordx4 v205, s[48:51], s66 offen offset:1024 nt lds\n"
    "ds_write_b128 v172, v[156:159] offset:12288\n"
    "v_mfma_f32_16x16x32_bf16 v[32:35], a[232:235], a[20:23], v[32:35]\n"
    "v_exp_f32 v180, v124\n"
    "v_mfma_f32_16x16x32_bf16 v[36:39], a[236:239], a[12:15], v[36:39]\n"
    "v_exp_f32 v181, v125\n"
    "v_mfma_f32_16x16x32_bf16 v[40:43], a[236:239], a[16:19], v[40:43]\n"
    "v_add_f32 v218, v218, v187\n"
    "s_add_u32 m0, s91, 57344\n"
    "v_mfma_f32_16x16x32_bf16 v[44:47], a[236:239], a[20:23], v[44:47]\n"
    "s_waitcnt lgkmcnt(1)\n"
    "v_mfma_f32_16x16x32_bf16 v[48:51], a[240:243], a[12:15], v[48:51]\n"
    "v_exp_f32 v182, v126\n"
    "v_mfma_f32_16x16x32_bf16 v[52:55], a[240:243], a[16:19], v[52:55]\n"
    "buffer_load_dwordx4 v206, s[48:51], s66 offen offset:0 nt lds\n"
    "v_cvt_pk_bf16_f32 v160, v180, v181\n"
    "v_mfma_f32_16x16x32_bf16 v[56:59], a[240:243], a[20:23], v[56:59]\n"
    "v_add_f32 v219, v180, v181\n"
    "v_mfma_f32_16x16x32_bf16 v[60:63], a[244:247], a[12:15], v[60:63]\n"
    "v_exp_f32 v183, v127\n"
    "v_mfma_f32_16x16x32_bf16 v[64:67], a[244:247], a[16:19], v[64:67]\n"
    "s_waitcnt vmcnt(15) lgkmcnt(0)\n"
    "s_barrier\n"
    "v_mfma_f32_16x16x32_bf16 v[68:71], a[244:247], a[20:23], v[68:71]\n"
    "ds_read_b128 a[192:195], v188 offset:0\n"
    "ds_read_b128 a[196:199], v188 offset:8192\n"
    "v_mfma_f32_16x16x32_bf16 v[72:75], a[248:251], a[12:15], v[72:75]\n"
    "v_cvt_pk_bf16_f32 v161, v182, v183\n"
    "ds_read_b128 a[200:203], v189 offset:0\n"
    "v_mfma_f32_16x16x32_bf16 v[76:79], a[248:251], a[16:19], v[76:79]\n"
    "ds_read_b128 a[204:207], v189 offset:8192\n"
    "buffer_load_dwordx4 v207, s[48:51], s66 offen offset:1024 nt lds\n"
    "v_mfma_f32_16x16x32_bf16 v[80:83], a[248:251], a[20:23], v[80:83]\n"
    "v_add_f32 v219, v219, v182\n"
    "s_add_u32 s66, s66, 32768\n"
    "v_mfma_f32_16x16x32_bf16 v[84:87], a[252:255], a[12:15], v[84:87]\n"
    "v_add_f32 v219, v219, v183\n"
    "v_mfma_f32_16x16x32_bf16 v[88:91], a[252:255], a[16:19], v[88:91]\n"
    "v_mfma_f32_16x16x32_bf16 v[92:95], a[252:255], a[20:23], v[92:95]\n"
    "s_waitcnt lgkmcnt(2)\n"
    "s_sub_u32 s69, s69, 1\n"
    "s_cbranch_scc0 .Lal0_%=\n"
    "s_branch .Ltail0_%=\n"
    ".Lonce_%=:\n"
    "s_getpc_b64 s[38:39]\n"
    ".Ljit_%=:\n"
    "s_add_u32 s38, s38, .Ltail0_%=-.Ljit_%=\n"
    "s_addc_u32 s39, s39, 0\n"
    "s_lshl_b32 s76, s36, 1\n"
    "s_and_b32 s77, s37, 1\n"
    "s_add_u32 s76, s76, s77\n"
    "s_lshl_b32 s76, s76, 9\n"
    "v_min_u32 v239, 3, v254\n"
    "v_lshl_add_u32 v239, v239, 7, s76\n"
    "s_mul_i32 s76, s36, 14336\n"
    "s_and_b32 s77, s64, 1\n"
    "s_mul_i32 s77, s77, 7168\n"
    "s_add_u32 s76, s76, s77\n"
    "s_add_u32 s76, s56, s76\n"
    "s_addc_u32 s77, s57, 0\n"
    "v_min_u32 v232, 55, v254\n"
    "v_lshlrev_b32 v232, 7, v232\n"
    "s_cmp_ge_u32 s64, 2\n"
    "s_cselect_b32 s38, s76, s38\n"
    "s_cselect_b32 s39, s77, s39\n"
    "s_cselect_b64 vcc, -1, 0\n"
    "v_cndmask_b32 v239, v239, v232, vcc\n"
    "global_load_dword v255, v239, s[38:39] offset:0\n"
    "v_readfirstlane_b32 s96, v230\n"
    "v_readfirstlane_b32 s97, v231\n"
    "s_nop 3\n"
    "s_mul_i32 s98, s96, 0x9e3779b1\n"
    "s_add_u32 s98, s98, 0x7f4a7c15\n"
    "s_xor_b32 s99, s97, s98\n"
    "s_mul_i32 s99, s99, 0x85ebca77\n"
    "s_add_u32 s99, s99, 0x27d4eb2f\n"
    "s_mov_b64 s[40:41], %[pout]\n"
    "s_and_b32 s41, s41, 0xffff\n"
    "s_mov_b32 s42, 86016\n"
    "s_mov_b32 s43, 0x20000\n"
    "v_bfrev_b32 v237, 1\n"
    "s_lshl_b32 s76, s37, 2\n"
    "s_add_u32 s76, s76, s64\n"
    "s_mul_i32 s77, s76, 36\n"
    "v_add_u32 v232, s77, v254\n"
    "v_cmp_gt_u32 vcc, 36, v254\n"
    "v_lshlrev_b32 v236, 4, v232\n"
    "v_lshrrev_b32 v233, 6, v232\n"
    "v_cndmask_b32 v176, v237, v236, vcc\n"
    "v_mul_u32_u24 v234, 171, v233\n"
    "v_lshrrev_b32 v234, 9, v234\n"
    "v_and_b32 v236, 1, v234\n"
    "v_sub_u32 v236, v236, v234\n"
    "v_mul_i32_i24 v236, 3, v236\n"
    "v_add_u32 v235, v233, v236\n"
    "v_and_b32 v236, 15, v232\n"
    "v_lshl_add_u32 v235, v235, 4, v236\n"
    "v_mul_u32_u24 v235, 896, v235\n"
    "v_lshrrev_b32 v234, 1, v234\n"
    "v_lshl_add_u32 v235, v234, 6, v235\n"
    "v_and_b32 v236, 16, v232\n"
    "v_lshl_add_u32 v235, v236, 1, v235\n"
    "v_bfe_u32 v236, v232, 5, 1\n"
    "v_lshl_add_u32 v235, v236, 4, v235\n"
    "v_cndmask_b32 v178, v237, v235, vcc\n"
    "s_mul_i32 s77, s76, 48\n"
    "s_add_u32 s77, s77, 2304\n"
    "v_add_u32 v232, s77, v254\n"
    "v_cmp_gt_u32 vcc, 48, v254\n"
    "v_lshlrev_b32 v236, 4, v232\n"
    "v_lshrrev_b32 v233, 6, v232\n"
    "v_cndmask_b32 v177, v237, v236, vcc\n"
    "v_mul_u32_u24 v234, 171, v233\n"
    "v_lshrrev_b32 v234, 9, v234\n"
    "v_and_b32 v236, 1, v234\n"
    "v_sub_u32 v236, v236, v234\n"
    "v_mul_i32_i24 v236, 3, v236\n"
    "v_add_u32 v235, v233, v236\n"
    "v_and_b32 v236, 15, v232\n"
    "v_lshl_add_u32 v235, v235, 4, v236\n"
    "v_mul_u32_u24 v235, 896, v235\n"
    "v_lshrrev_b32 v234, 1, v234\n"
    "v_lshl_add_u32 v235, v234, 6, v235\n"
    "v_and_b32 v236, 16, v232\n"
    "v_lshl_add_u32 v235, v236, 1, v235\n"
    "v_bfe_u32 v236, v232, 5, 1\n"
    "v_lshl_add_u32 v235, v236, 4, v235\n"
    "v_cndmask_b32 v179, v237, v235, vcc\n"
    "s_branch .Lonceb_%=\n"
    ".Ltail0_%=:\n"
    "v_mfma_f32_16x16x32_bf16 v[96:99], a[192:195], a[96:99], v[144:147]\n"
    "s_add_u32 m0, s91, 65536\n"
    "ds_read_b128 a[208:211], v190 offset:0\n"
    "v_mfma_f32_16x16x32_bf16 v[100:103], a[192:195], a[128:131], v[148:151]\n"
    "buffer_load_dwordx4 v208, s[52:55], s67 offen offset:0 nt lds\n"
    "ds_read_b128 a[212:215], v190 offset:8192\n"
    "v_mfma_f32_16x16x32_bf16 v[104:107], a[192:195], a[160:163], v[152:155]\n"
    "v_exp_f32 v184, v136\n"
    "v_mfma_f32_16x16x32_bf16 v[108:111], a[196:199], a[96:99], v[144:147]\n"
    "ds_read_b128 a[216:219], v191 offset:0\n"
    "ds_read_b128 a[220:223], v191 offset:8192\n"
    "v_mfma_f32_16x16x32_bf16 v[112:115], a[196:199], a[128:131], v[148:151]\n"
    "v_exp_f32 v185, v137\n"
    "v_mfma_f32_16x16x32_bf16 v[116:119], a[196:199], a[160:163], v[152:155]\n"
    "ds_read_b128 a[192:195], v188 offset:256\n"
    "s_waitcnt lgkmcnt(5)\n"
    "v_mfma_f32_16x16x32_bf16 v[96:99], a[200:203], a[100:103], v[96:99]\n"
    "v_exp_f32 v186, v138\n"
    "v_cvt_pk_bf16_f32 v162, v184, v185\n"
    "v_mfma_f32_16x16x32_bf16 v[100:103], a[200:203], a[132:135], v[100:103]\n"
    "v_add_f32 v219, v219, v184\n"
    "buffer_load_dwordx4 v209, s[52:55], s67 offen offset:1024 nt lds\n"
    "v_mfma_f32_16x16x32_bf16 v[104:107], a[200:203], a[164:167], v[104:107]\n"
    "v_add_f32 v219, v219, v185\n"
    "ds_read_b128 a[196:199], v188 offset:8448\n"
    "v_mfma_f32_16x16x32_bf16 v[108:111], a[204:207], a[100:103], v[108:111]\n"
    "v_exp_f32 v187, v139\n"
    "v_mfma_f32_16x16x32_bf16 v[112:115], a[204:207], a[132:135], v[112:115]\n"
    "v_cvt_pk_bf16_f32 v163, v186, v187\n"
    "v_add_f32 v219, v219, v186\n"
    "v_mfma_f32_16x16x32_bf16 v[116:119], a[204:207], a[164:167], v[116:119]\n"
    "s_add_u32 m0, s91, 73728\n"
    "s_waitcnt lgkmcnt(2)\n"
    "v_mfma_f32_16x16x32_bf16 v[96:99], a[208:211], a[104:107], v[96:99]\n"
    "ds_write_b128 v172, v[160:163] offset:13312\n"
    "v_add_f32 v219, v219, v187\n"
    "v_mfma_f32_16x16x32_bf16 v[100:103], a[208:211], a[136:139], v[100:103]\n"
    "buffer_load_dwordx4 v210, s[52:55], s67 offen offset:0 nt lds\n"
    "ds_read_b128 a[200:203], v189 offset:256\n"
    "v_mfma_f32_16x16x32_bf16 v[104:107], a[208:211], a[168:171], v[104:107]\n"
    "v_exp_f32 v180, v128\n"
    "v_mfma_f32_16x16x32_bf16 v[108:111], a[212:215], a[104:107], v[108:111]\n"
    "ds_read_b128 a[204:207], v189 offset:8448\n"
    "ds_read_b128 a[208:211], v190 offset:256\n"
    "v_mfma_f32_16x16x32_bf16 v[112:115], a[212:215], a[136:139], v[112:115]\n"
    "v_exp_f32 v181, v129\n"
    "v_mfma_f32_16x16x32_bf16 v[116:119], a[212:215], a[168:171], v[116:119]\n"
    "v_exp_f32 v182, v130\n"
    "v_cvt_pk_bf16_f32 v164, v180, v181\n"
    "v_mfma_f32_16x16x32_bf16 v[96:99], a[216:219], a[108:111], v[96:99]\n"
    "v_add_f32 v220, v180, v181\n"
    "ds_read_b128 a[212:215], v190 offset:8448\n"
    "v_mfma_f32_16x16x32_bf16 v[100:103], a[216:219], a[140:143], v[100:103]\n"
    "buffer_load_dwordx4 v211, s[52:55], s67 offen offset:1024 nt lds\n"
    "v_mfma_f32_16x16x32_bf16 v[104:107], a[216:219], a[172:175], v[104:107]\n"
    "v_exp_f32 v183, v131\n"
    "s_nop 0\n"
    "v_cvt_pk_bf16_f32 v165, v182, v183\n"
    "v_mfma_f32_16x16x32_bf16 v[108:111], a[220:223], a[108:111], v[108:111]\n"
    "v_add_f32 v220, v220, v182\n"
    "v_add_f32 v220, v220, v183\n"
    "v_mfma_f32_16x16x32_bf16 v[112:115], a[220:223], a[140:143], v[112:115]\n"
    "s_add_u32 m0, s91, 81920\n"
    "ds_read_b128 a[216:219], v191 offset:256\n"
    "v_mfma_f32_16x16x32_bf16 v[116:119], a[220:223], a[172:175], v[116:119]\n"
    "s_waitcnt lgkmcnt(3)\n"
    "v_mfma_f32_16x16x32_bf16 v[96:99], a[192:195], a[112:115], v[96:99]\n"
    "v_exp_f32 v184, v140\n"
    "v_mfma_f32_16x16x32_bf16 v[100:103], a[192:195], a[144:147], v[100:103]\n"
    "buffer_load_dwordx4 v208, s[52:55], s68 offen offset:0 nt lds\n"
    "ds_read_b128 a[220:223], v191 offset:8448\n"
    "v_mfma_f32_16x16x32_bf16 v[104:107], a[192:195], a[176:179], v[104:107]\n"
    "v_exp_f32 v185, v141\n"
    "v_mfma_f32_16x16x32_bf16 v[108:111], a[196:199], a[112:115], v[108:111]\n"
    "v_exp_f32 v186, v142\n"
    "v_cvt_pk_bf16_f32 v166, v184, v185\n"
    "v_add_f32 v220, v220, v184\n"
    "v_mfma_f32_16x16x32_bf16 v[112:115], a[196:199], a[144:147], v[112:115]\n"
    "v_add_f32 v220, v220, v185\n"
    "v_mfma_f32_16x16x32_bf16 v[116:119], a[196:199], a[176:179], v[116:119]\n"
    "v_exp_f32 v187, v143\n"
    "s_nop 0\n"
    "v_cvt_pk_bf16_f32 v167, v186, v187\n"
    "v_mfma_f32_16x16x32_bf16 v[96:99], a[200:203], a[116:119], v[96:99]\n"
    "v_add_f32 v220, v220, v186\n"
    "ds_write_b128 v172, v[164:167] offset:14336\n"
    "v_mfma_f32_16x16x32_bf16 v[100:103], a[200:203], a[148:151], v[100:103]\n"
    "buffer_load_dwordx4 v209, s[52:55], s68 offen offset:1024 nt lds\n"
    "v_add_f32 v220, v220, v187\n"
    "v_mfma_f32_16x16x32_bf16 v[104:107], a[200:203], a[180:183], v[104:107]\n"
    "v_max3_f32 v221, v218, v219, v220\n"
    "v_mfma_f32_16x16x32_bf16 v[108:111], a[204:207], a[116:119], v[108:111]\n"
    "v_cmp_lt_f32 vcc, s70, v221\n"
    "s_cbranch_vccnz .Lfw2_%=\n"
    ".Lfb2_%=:\n"
    "v_mfma_f32_16x16x32_bf16 v[112:115], a[204:207], a[148:151], v[112:115]\n"
    "s_add_u32 m0, s91, 90112\n"
    "v_mfma_f32_16x16x32_bf16 v[116:119], a[204:207], a[180:183], v[116:119]\n"
    "s_waitcnt lgkmcnt(1)\n"
    "v_mfma_f32_16x16x32_bf16 v[96:99], a[208:211], a[120:123], v[96:99]\n"
    "v_mfma_f32_16x16x32_bf16 v[100:103], a[208:211], a[152:155], v[100:103]\n"
    "buffer_load_dwordx4 v210, s[52:55], s68 offen offset:0 nt lds\n"
    "v_mfma_f32_16x16x32_bf16 v[104:107], a[208:211], a[184:187], v[104:107]\n"
    "v_mfma_f32_16x16x32_bf16 v[108:111], a[212:215], a[120:123], v[108:111]\n"
    "v_mfma_f32_16x16x32_bf16 v[112:115], a[212:215], a[152:155], v[112:115]\n"
    "s_waitcnt vmcnt(15) lgkmcnt(0)\n"
    "s_barrier\n"
    "v_mfma_f32_16x16x32_bf16 v[116:119], a[212:215], a[184:187], v[116:119]\n"
    "ds_read_b32 v168, v175 offset:0\n"
    "ds_read_b128 a[0:3], v173 offset:12288\n"
    "v_mfma_f32_16x16x32_bf16 v[96:99], a[216:219], a[124:127], v[96:99]\n"
    "ds_read_b128 a[4:7], v173 offset:13312\n"
    "ds_read_b128 a[8:11], v173 offset:14336\n"
    "v_mfma_f32_16x16x32_bf16 v[100:103], a[216:219], a[156:159], v[100:103]\n"
    "ds_read_b64_tr_b16 a[224:225], v192 offset:32768\n"
    "ds_read_b64_tr_b16 a[226:227], v192 offset:40960\n"
    "v_mfma_f32_16x16x32_bf16 v[104:107], a[216:219], a[188:191], v[104:107]\n"
    "ds_read_b64_tr_b16 a[228:229], v193 offset:32768\n"
    "ds_read_b64_tr_b16 a[230:231], v193 offset:40960\n"
    "buffer_load_dwordx4 v211, s[52:55], s68 offen offset:1024 nt lds\n"
    "v_mfma_f32_16x16x32_bf16 v[108:111], a[220:223], a[124:127], v[108:111]\n"
    "ds_read_b64_tr_b16 a[232:233], v194 offset:32768\n"
    "ds_read_b64_tr_b16 a[234:235], v194 offset:40960\n"
    "s_waitcnt lgkmcnt(9)\n"
    "v_cmp_ne_u32 vcc, 0, v168\n"
    "s_cbranch_vccnz .Ltr02_%=\n"
    ".Lbk02_%=:\n"
    "v_mfma_f32_16x16x32_bf16 v[112:115], a[220:223], a[156:159], v[112:115]\n"
    "ds_read_b64_tr_b16 a[236:237], v195 offset:32768\n"
    "ds_read_b64_tr_b16 a[238:239], v195 offset:40960\n"
    "s_add_u32 s67, s67, 32768\n"
    "s_add_u32 s68, s68, 32768\n"
    "v_mfma_f32_16x16x32_bf16 v[116:119], a[220:223], a[188:191], v[116:119]\n"
    "s_waitcnt lgkmcnt(4)\n"
    "v_mfma_f32_16x16x32_bf16 v[0:3], a[224:227], a[0:3], v[0:3]\n"
    "s_add_u32 m0, s91, 0\n"
    "ds_read_b64_tr_b16 a[240:241], v196 offset:32768\n"
    "v_mfma_f32_16x16x32_bf16 v[4:7], a[224:227], a[4:7], v[4:7]\n"
    "ds_read_b64_tr_b16 a[242:243], v196 offset:40960\n"
    "buffer_load_dwordx4 v208, s[52:55], s67 offen offset:0 nt lds\n"
    "v_mfma_f32_16x16x32_bf16 v[8:11], a[224:227], a[8:11], v[8:11]\n"
    "ds_read_b64_tr_b16 a[244:245], v197 offset:32768\n"
    "ds_read_b64_tr_b16 a[246:247], v197 offset:40960\n"
    "v_mfma_f32_16x16x32_bf16 v[12:15], a[228:231], a[0:3], v[12:15]\n"
    "ds_read_b64_tr_b16 a[248:249], v198 offset:32768\n"
    "ds_read_b64_tr_b16 a[250:251], v198 offset:40960\n"
    "v_mfma_f32_16x16x32_bf16 v[16:19], a[228:231], a[4:7], v[16:19]\n"
    "v_exp_f32 v180, v96\n"
    "v_mfma_f32_16x16x32_bf16 v[20:23], a[228:231], a[8:11], v[20:23]\n"
    "ds_read_b128 a[12:15], v173 offset:18432\n"
    "ds_read_b128 a[16:19], v173 offset:19456\n"
    "ds_read_b128 a[20:23], v173 offset:20480\n"
    "ds_read_b64_tr_b16 a[252:253], v199 offset:32768\n"
    "ds_read_b64_tr_b16 a[254:255], v199 offset:40960\n"
    "s_waitcnt lgkmcnt(11)\n"
    "v_mfma_f32_16x16x32_bf16 v[24:27], a[232:235], a[0:3], v[24:27]\n"
    "v_add_f32 v212, v212, v218\n"
    "v_add_f32 v213, v213, v219\n"
    "v_mfma_f32_16x16x32_bf16 v[28:31], a[232:235], a[4:7], v[28:31]\n"
    "buffer_load_dwordx4 v209, s[52:55], s67 offen offset:1024 nt lds\n"
    "v_add_f32 v214, v214, v220\n"
    "v_mfma_f32_16x16x32_bf16 v[32:35], a[232:235], a[8:11], v[32:35]\n"
    "v_exp_f32 v181, v97\n"
    "v_mfma_f32_16x16x32_bf16 v[36:39], a[236:239], a[0:3], v[36:39]\n"
    "v_exp_f32 v182, v98\n"
    "v_mfma_f32_16x16x32_bf16 v[40:43], a[236:239], a[4:7], v[40:43]\n"
    "s_add_u32 m0, s91, 8192\n"
    "v_cvt_pk_bf16_f32 v156, v180, v181\n"
    "v_mfma_f32_16x16x32_bf16 v[44:47], a[236:239], a[8:11], v[44:47]\n"
    "ds_read_b64_tr_b16 a[224:225], v192 offset:49152\n"
    "s_waitcnt lgkmcnt(1)\n"
    "v_mfma_f32_16x16x32_bf16 v[48:51], a[240:243], a[0:3], v[48:51]\n"
    "ds_read_b64_tr_b16 a[226:227], v192 offset:57344\n"
    "ds_read_b64_tr_b16 a[228:229], v193 offset:49152\n"
    "v_mfma_f32_16x16x32_bf16 v[52:55], a[240:243], a[4:7], v[52:55]\n"
    "buffer_load_dwordx4 v210, s[52:55], s67 offen offset:0 nt lds\n"
    "ds_read_b64_tr_b16 a[230:231], v193 offset:57344\n"
    "v_mfma_f32_16x16x32_bf16 v[56:59], a[240:243], a[8:11], v[56:59]\n"
    "v_exp_f32 v183, v99\n"
    "v_mfma_f32_16x16x32_bf16 v[60:63], a[244:247], a[0:3], v[60:63]\n"
    "v_add_f32 v215, v180, v181\n"
    "ds_read_b64_tr_b16 a[232:233], v194 offset:49152\n"
    "v_mfma_f32_16x16x32_bf16 v[64:67], a[244:247], a[4:7], v[64:67]\n"
    "ds_read_b64_tr_b16 a[234:235], v194 offset:57344\n"
    "ds_read_b64_tr_b16 a[236:237], v195 offset:49152\n"
    "v_mfma_f32_16x16x32_bf16 v[68:71], a[244:247], a[8:11], v[68:71]\n"
    "ds_read_b64_tr_b16 a[238:239], v195 offset:57344\n"
    "v_cvt_pk_bf16_f32 v157, v182, v183\n"
    "v_mfma_f32_16x16x32_bf16 v[72:75], a[248:251], a[0:3], v[72:75]\n"
    "v_exp_f32 v184, v108\n"
    "v_mfma_f32_16x16x32_bf16 v[76:79], a[248:251], a[4:7], v[76:79]\n"
    "v_add_f32 v215, v215, v182\n"
    "buffer_load_dwordx4 v211, s[52:55], s67 offen offset:1024 nt lds\n"
    "v_mfma_f32_16x16x32_bf16 v[80:83], a[248:251], a[8:11], v[80:83]\n"
    "v_add_f32 v215, v215, v183\n"
    "ds_read_b64_tr_b16 a[240:241], v196 offset:49152\n"
    "v_mfma_f32_16x16x32_bf16 v[84:87], a[252:255], a[0:3], v[84:87]\n"
    "v_exp_f32 v185, v109\n"
    "v_mfma_f32_16x16x32_bf16 v[88:91], a[252:255], a[4:7], v[88:91]\n"
    "s_add_u32 m0, s91, 16384\n"
    "ds_read_b64_tr_b16 a[242:243], v196 offset:57344\n"
    "v_mfma_f32_16x16x32_bf16 v[92:95], a[252:255], a[8:11], v[92:95]\n"
    "ds_read_b64_tr_b16 a[244:245], v197 offset:49152\n"
    "s_waitcnt lgkmcnt(3)\n"
    "v_mfma_f32_16x16x32_bf16 v[0:3], a[224:227], a[12:15], v[0:3]\n"
    "v_exp_f32 v186, v110\n"
    "v_cvt_pk_bf16_f32 v158, v184, v185\n"
    "v_mfma_f32_16x16x32_bf16 v[4:7], a[224:227], a[16:19], v[4:7]\n"
    "buffer_load_dwordx4 v208, s[52:55], s68 offen offset:0 nt lds\n"
    "v_add_f32 v215, v215, v184\n"
    "v_mfma_f32_16x16x32_bf16 v[8:11], a[224:227], a[20:23], v[8:11]\n"
    "ds_read_b64_tr_b16 a[246:247], v197 offset:57344\n"
    "ds_read_b64_tr_b16 a[248:249], v198 offset:49152\n"
    "v_mfma_f32_16x16x32_bf16 v[12:15], a[228:231], a[12:15], v[12:15]\n"
    "ds_read_b64_tr_b16 a[250:251], v198 offset:57344\n"
    "v_add_f32 v215, v215, v185\n"
    "v_mfma_f32_16x16x32_bf16 v[16:19], a[228:231], a[16:19], v[16:19]\n"
    "ds_read_b64_tr_b16 a[252:253], v199 offset:49152\n"
    "ds_read_b64_tr_b16 a[254:255], v199 offset:57344\n"
    "v_mfma_f32_16x16x32_bf16 v[20:23], a[228:231], a[20:23], v[20:23]\n"
    "v_exp_f32 v187, v111\n"
    "v_mfma_f32_16x16x32_bf16 v[24:27], a[232:235], a[12:15], v[24:27]\n"
    "v_cvt_pk_bf16_f32 v159, v186, v187\n"
    "v_add_f32 v215, v215, v186\n"
    "v_mfma_f32_16x16x32_bf16 v[28:31], a[232:235], a[16:19], v[28:31]\n"
    "buffer_load_dwordx4 v209, s[52:55], s68 offen offset:1024 nt lds\n"
    "ds_write_b128 v172, v[156:159] offset:0\n"
    "v_mfma_f32_16x16x32_bf16 v[32:35], a[232:235], a[20:23], v[32:35]\n"
    "v_exp_f32 v180, v100\n"
    "v_mfma_f32_16x16x32_bf16 v[36:39], a[236:239], a[12:15], v[36:39]\n"
    "v_exp_f32 v181, v101\n"
    "v_mfma_f32_16x16x32_bf16 v[40:43], a[236:239], a[16:19], v[40:43]\n"
    "v_add_f32 v215, v215, v187\n"
    "s_add_u32 m0, s91, 24576\n"
    "v_mfma_f32_16x16x32_bf16 v[44:47], a[236:239], a[20:23], v[44:47]\n"
    "s_waitcnt lgkmcnt(1)\n"
    "v_mfma_f32_16x16x32_bf16 v[48:51], a[240:243], a[12:15], v[48:51]\n"
    "v_exp_f32 v182, v102\n"
    "v_mfma_f32_16x16x32_bf16 v[52:55], a[240:243], a[16:19], v[52:55]\n"
    "buffer_load_dwordx4 v210, s[52:55], s68 offen offset:0 nt lds\n"
    "v_cvt_pk_bf16_f32 v160, v180, v181\n"
    "v_mfma_f32_16x16x32_bf16 v[56:59], a[240:243], a[20:23], v[56:59]\n"
    "v_add_f32 v216, v180, v181\n"
    "v_mfma_f32_16x16x32_bf16 v[60:63], a[244:247], a[12:15], v[60:63]\n"
    "v_exp_f32 v183, v103\n"
    "v_mfma_f32_16x16x32_bf16 v[64:67], a[244:247], a[16:19], v[64:67]\n"
    "s_waitcnt vmcnt(15) lgkmcnt(0)\n"
    "s_barrier\n"
    "v_mfma_f32_16x16x32_bf16 v[68:71], a[244:247], a[20:23], v[68:71]\n"
    "ds_read_b128 a[192:195], v188 offset:32768\n"
    "ds_read_b128 a[196:199], v188 offset:40960\n"
    "v_mfma_f32_16x16x32_bf16 v[72:75], a[248:251], a[12:15], v[72:75]\n"
    "v_cvt_pk_bf16_f32 v161, v182, v183\n"
    "ds_read_b128 a[200:203], v189 offset:32768\n"
    "v_mfma_f32_16x16x32_bf16 v[76:79], a[248:251], a[16:19], v[76:79]\n"
    "ds_read_b128 a[204:207], v189 offset:40960\n"
    "buffer_load_dwordx4 v211, s[52:55], s68 offen offset:1024 nt lds\n"
    "v_mfma_f32_16x16x32_bf16 v[80:83], a[248:251], a[20:23], v[80:83]\n"
    "v_add_f32 v216, v216, v182\n"
    "s_add_u32 s67, s67, 32768\n"
    "v_mfma_f32_16x16x32_bf16 v[84:87], a[252:255], a[12:15], v[84:87]\n"
    "s_add_u32 s68, s68, 32768\n"
    "v_add_f32 v216, v216, v183\n"
    "v_mfma_f32_16x16x32_bf16 v[88:91], a[252:255], a[16:19], v[88:91]\n"
    "v_mfma_f32_16x16x32_bf16 v[92:95], a[252:255], a[20:23], v[92:95]\n"
    "s_waitcnt lgkmcnt(2)\n"
    "v_mfma_f32_16x16x32_bf16 v[120:123], a[192:195], a[96:99], v[144:147]\n"
    "s_mul_i32 s66, s64, 57344\n"
    "s_add_u32 m0, s65, 98304\n"
    "v_mfma_f32_16x16x32_bf16 v[124:127], a[192:195], a[128:131], v[148:151]\n"
    "ds_read_b128 a[208:211], v190 offset:32768\n"
    "buffer_load_dwordx4 v240, s[56:59], s66 offen offset:0 lds\n"
    "v_mfma_f32_16x16x32_bf16 v[128:131], a[192:195], a[160:163], v[152:155]\n"
    "v_exp_f32 v184, v112\n"
    "v_mfma_f32_16x16x32_bf16 v[132:135], a[196:199], a[96:99], v[144:147]\n"
    "ds_read_b128 a[212:215], v190 offset:40960\n"
    "ds_read_b128 a[216:219], v191 offset:32768\n"
    "v_mfma_f32_16x16x32_bf16 v[136:139], a[196:199], a[128:131], v[148:151]\n"
    "ds_read_b128 a[220:223], v191 offset:40960\n"
    "ds_read_b128 a[192:195], v188 offset:33024\n"
    "v_mfma_f32_16x16x32_bf16 v[140:143], a[196:199], a[160:163], v[152:155]\n"
    "v_exp_f32 v185, v113\n"
    "s_waitcnt lgkmcnt(5)\n"
    "v_mfma_f32_16x16x32_bf16 v[120:123], a[200:203], a[100:103], v[120:123]\n"
    "v_exp_f32 v186, v114\n"
    "v_cvt_pk_bf16_f32 v162, v184, v185\n"
    "v_mfma_f32_16x16x32_bf16 v[124:127], a[200:203], a[132:135], v[124:127]\n"
    "v_add_f32 v216, v216, v184\n"
    "buffer_load_dwordx4 v241, s[56:59], s66 offen offset:1024 lds\n"
    "v_mfma_f32_16x16x32_bf16 v[128:131], a[200:203], a[164:167], v[128:131]\n"
    "v_add_f32 v216, v216, v185\n"
    "ds_read_b128 a[196:199], v188 offset:41216\n"
    "v_mfma_f32_16x16x32_bf16 v[132:135], a[204:207], a[100:103], v[132:135]\n"
    "v_exp_f32 v187, v115\n"
    "v_mfma_f32_16x16x32_bf16 v[136:139], a[204:207], a[132:135], v[136:139]\n"
    "v_cvt_pk_bf16_f32 v163, v186, v187\n"
    "v_add_f32 v216, v216, v186\n"
    "v_mfma_f32_16x16x32_bf16 v[140:143], a[204:207], a[164:167], v[140:143]\n"
    "ds_write_b128 v172, v[160:163] offset:1024\n"
    "s_waitcnt lgkmcnt(3)\n"
    "v_mfma_f32_16x16x32_bf16 v[120:123], a[208:211], a[104:107], v[120:123]\n"
    "v_exp_f32 v180, v104\n"
    "v_mfma_f32_16x16x32_bf16 v[124:127], a[208:211], a[136:139], v[124:127]\n"
    "buffer_load_dwordx4 v242, s[56:59], s66 offen offset:2048 lds\n"
    "v_add_f32 v216, v216, v187\n"
    "v_mfma_f32_16x16x32_bf16 v[128:131], a[208:211], a[168:171], v[128:131]\n"
    "ds_read_b128 a[200:203], v189 offset:33024\n"
    "ds_read_b128 a[204:207], v189 offset:41216\n"
    "v_mfma_f32_16x16x32_bf16 v[132:135], a[212:215], a[104:107], v[132:135]\n"
    "v_exp_f32 v181, v105\n"
    "v_mfma_f32_16x16x32_bf16 v[136:139], a[212:215], a[136:139], v[136:139]\n"
    "v_exp_f32 v182, v106\n"
    "v_cvt_pk_bf16_f32 v164, v180, v181\n"
    "v_mfma_f32_16x16x32_bf16 v[140:143], a[212:215], a[168:171], v[140:143]\n"
    "v_add_f32 v217, v180, v181\n"
    "ds_read_b128 a[208:211], v190 offset:33024\n"
    "v_mfma_f32_16x16x32_bf16 v[120:123], a[216:219], a[108:111], v[120:123]\n"
    "v_exp_f32 v183, v107\n"
    "v_mfma_f32_16x16x32_bf16 v[124:127], a[216:219], a[140:143], v[124:127]\n"
    "buffer_load_dwordx4 v243, s[56:59], s66 offen offset:3072 lds\n"
    "v_cvt_pk_bf16_f32 v165, v182, v183\n"
    "v_mfma_f32_16x16x32_bf16 v[128:131], a[216:219], a[172:175], v[128:131]\n"
    "v_add_f32 v217, v217, v182\n"
    "v_add_f32 v217, v217, v183\n"
    "v_mfma_f32_16x16x32_bf16 v[132:135], a[220:223], a[108:111], v[132:135]\n"
    "v_exp_f32 v184, v116\n"
    "v_mfma_f32_16x16x32_bf16 v[136:139], a[220:223], a[140:143], v[136:139]\n"
    "s_add_u32 m0, s65, 102400\n"
    "ds_read_b128 a[212:215], v190 offset:41216\n"
    "v_mfma_f32_16x16x32_bf16 v[140:143], a[220:223], a[172:175], v[140:143]\n"
    "ds_read_b128 a[216:219], v191 offset:33024\n"
    "s_waitcnt lgkmcnt(3)\n"
    "v_mfma_f32_16x16x32_bf16 v[120:123], a[192:195], a[112:115], v[120:123]\n"
    "v_exp_f32 v185, v117\n"
    "v_mfma_f32_16x16x32_bf16 v[124:127], a[192:195], a[144:147], v[124:127]\n"
    "buffer_load_dwordx4 v244, s[56:59], s66 offen offset:0 lds\n"
    "ds_read_b128 a[220:223], v191 offset:41216\n"
    "v_mfma_f32_16x16x32_bf16 v[128:131], a[192:195], a[176:179], v[128:131]\n"
    "v_exp_f32 v186, v118\n"
    "v_cvt_pk_bf16_f32 v166, v184, v185\n"
    "v_add_f32 v217, v217, v184\n"
    "v_mfma_f32_16x16x32_bf16 v[132:135], a[196:199], a[112:115], v[132:135]\n"
    "v_add_f32 v217, v217, v185\n"
    "s_mul_i32 s71, s37, 86016\n"
    "v_mfma_f32_16x16x32_bf16 v[136:139], a[196:199], a[144:147], v[136:139]\n"
    "v_exp_f32 v187, v119\n"
    "v_mfma_f32_16x16x32_bf16 v[140:143], a[196:199], a[176:179], v[140:143]\n"
    "v_cvt_pk_bf16_f32 v167, v186, v187\n"
    "v_add_f32 v217, v217, v186\n"
    "v_mfma_f32_16x16x32_bf16 v[120:123], a[200:203], a[116:119], v[120:123]\n"
    "ds_write_b128 v172, v[164:167] offset:2048\n"
    "v_add_f32 v217, v217, v187\n"
    "v_mfma_f32_16x16x32_bf16 v[124:127], a[200:203], a[148:151], v[124:127]\n"
    "buffer_load_dwordx4 v245, s[56:59], s66 offen offset:1024 lds\n"
    "v_max3_f32 v221, v215, v216, v217\n"
    "v_mfma_f32_16x16x32_bf16 v[128:131], a[200:203], a[180:183], v[128:131]\n"
    "v_cmp_lt_f32 vcc, s70, v221\n"
    "s_cbranch_vccnz .Lfw3_%=\n"
    ".Lfb3_%=:\n"
    "v_mfma_f32_16x16x32_bf16 v[132:135], a[204:207], a[116:119], v[132:135]\n"
    "s_mul_i32 s76, s64, 3072\n"
    "s_add_u32 s71, s71, s76\n"
    "v_mfma_f32_16x16x32_bf16 v[136:139], a[204:207], a[148:151], v[136:139]\n"
    "s_mov_b64 s[44:45], %[pf]\n"
    "s_and_b32 s45, s45, 0xffff\n"
    "v_mfma_f32_16x16x32_bf16 v[140:143], a[204:207], a[180:183], v[140:143]\n"
    "s_mov_b32 s46, 512\n"
    "s_waitcnt lgkmcnt(1)\n"
    "v_mfma_f32_16x16x32_bf16 v[120:123], a[208:211], a[120:123], v[120:123]\n"
    "s_mov_b32 s47, 0x20000\n"
    "v_mov_b32 v210, s98\n"
    "v_mfma_f32_16x16x32_bf16 v[124:127], a[208:211], a[152:155], v[124:127]\n"
    "buffer_load_dwordx4 v246, s[56:59], s66 offen offset:2048 lds\n"
    "v_mov_b32 v211, s99\n"
    "v_mfma_f32_16x16x32_bf16 v[128:131], a[208:211], a[184:187], v[128:131]\n"
    "s_lshl_b32 s76, s37, 3\n"
    "v_mov_b32 v209, s76\n"
    "v_mfma_f32_16x16x32_bf16 v[132:135], a[212:215], a[120:123], v[132:135]\n"
    "v_mfma_f32_16x16x32_bf16 v[136:139], a[212:215], a[152:155], v[136:139]\n"
    "s_waitcnt vmcnt(15) lgkmcnt(0)\n"
    "s_barrier\n"
    "v_mfma_f32_16x16x32_bf16 v[140:143], a[212:215], a[184:187], v[140:143]\n"
    "ds_read_b32 v168, v175 offset:0\n"
    "ds_read_b128 a[0:3], v173 offset:0\n"
    "v_mfma_f32_16x16x32_bf16 v[120:123], a[216:219], a[124:127], v[120:123]\n"
    "ds_read_b128 a[4:7], v173 offset:1024\n"
    "ds_read_b128 a[8:11], v173 offset:2048\n"
    "v_mfma_f32_16x16x32_bf16 v[124:127], a[216:219], a[156:159], v[124:127]\n"
    "ds_read_b64_tr_b16 a[224:225], v192 offset:0\n"
    "ds_read_b64_tr_b16 a[226:227], v192 offset:8192\n"
    "v_mfma_f32_16x16x32_bf16 v[128:131], a[216:219], a[188:191], v[128:131]\n"
    "ds_read_b64_tr_b16 a[228:229], v193 offset:0\n"
    "ds_read_b64_tr_b16 a[230:231], v193 offset:8192\n"
    "buffer_load_dwordx4 v247, s[56:59], s66 offen offset:3072 lds\n"
    "v_mfma_f32_16x16x32_bf16 v[132:135], a[220:223], a[124:127], v[132:135]\n"
    "ds_read_b64_tr_b16 a[232:233], v194 offset:0\n"
    "ds_read_b64_tr_b16 a[234:235], v194 offset:8192\n"
    "s_waitcnt lgkmcnt(9)\n"
    "v_cmp_ne_u32 vcc, 0, v168\n"
    "s_cbranch_vccnz .Ltr03_%=\n"
    ".Lbk03_%=:\n"
    "v_mfma_f32_16x16x32_bf16 v[136:139], a[220:223], a[156:159], v[136:139]\n"
    "ds_read_b64_tr_b16 a[236:237], v195 offset:0\n"
    "ds_read_b64_tr_b16 a[238:239], v195 offset:8192\n"
    "s_add_u32 s66, s66, 128\n"
    "v_mfma_f32_16x16x32_bf16 v[140:143], a[220:223], a[188:191], v[140:143]\n"
    "s_waitcnt lgkmcnt(4)\n"
    "v_mfma_f32_16x16x32_bf16 v[0:3], a[224:227], a[0:3], v[0:3]\n"
    "ds_read_b64_tr_b16 a[240:241], v196 offset:0\n"
    "ds_read_b64_tr_b16 a[242:243], v196 offset:8192\n"
    "ds_read_b64_tr_b16 a[244:245], v197 offset:0\n"
    "v_mfma_f32_16x16x32_bf16 v[4:7], a[224:227], a[4:7], v[4:7]\n"
    "v_exp_f32 v180, v120\n"
    "ds_read_b64_tr_b16 a[246:247], v197 offset:8192\n"
    "v_mfma_f32_16x16x32_bf16 v[8:11], a[224:227], a[8:11], v[8:11]\n"
    "v_exp_f32 v181, v121\n"
    "ds_read_b64_tr_b16 a[248:249], v198 offset:0\n"
    "v_mfma_f32_16x16x32_bf16 v[12:15], a[228:231], a[0:3], v[12:15]\n"
    "ds_read_b64_tr_b16 a[250:251], v198 offset:8192\n"
    "ds_read_b128 a[12:15], v173 offset:6144\n"
    "ds_read_b128 a[16:19], v173 offset:7168\n"
    "v_mfma_f32_16x16x32_bf16 v[16:19], a[228:231], a[4:7], v[16:19]\n"
    "ds_read_b128 a[20:23], v173 offset:8192\n"
    "ds_read_b64_tr_b16 a[252:253], v199 offset:0\n"
    "ds_read_b64_tr_b16 a[254:255], v199 offset:8192\n"
    "v_mfma_f32_16x16x32_bf16 v[20:23], a[228:231], a[8:11], v[20:23]\n"
    "v_exp_f32 v182, v122\n"
    "v_add_f32 v212, v212, v215\n"
    "s_waitcnt lgkmcnt(11)\n"
    "v_mfma_f32_16x16x32_bf16 v[24:27], a[232:235], a[0:3], v[24:27]\n"
    "v_exp_f32 v183, v123\n"
    "v_add_f32 v213, v213, v216\n"
    "v_mfma_f32_16x16x32_bf16 v[28:31], a[232:235], a[4:7], v[28:31]\n"
    "v_exp_f32 v184, v132\n"
    "v_add_f32 v214, v214, v217\n"
    "v_mfma_f32_16x16x32_bf16 v[32:35], a[232:235], a[8:11], v[32:35]\n"
    "v_cvt_pk_bf16_f32 v156, v180, v181\n"
    "v_exp_f32 v185, v133\n"
    "v_mfma_f32_16x16x32_bf16 v[36:39], a[236:239], a[0:3], v[36:39]\n"
    "v_add_f32 v218, v180, v181\n"
    "v_cvt_pk_bf16_f32 v157, v182, v183\n"
    "v_add_f32 v218, v218, v182\n"
    "v_mfma_f32_16x16x32_bf16 v[40:43], a[236:239], a[4:7], v[40:43]\n"
    "v_exp_f32 v186, v134\n"
    "v_add_f32 v218, v218, v183\n"
    "v_mfma_f32_16x16x32_bf16 v[44:47], a[236:239], a[8:11], v[44:47]\n"
    "v_exp_f32 v187, v135\n"
    "s_waitcnt lgkmcnt(0)\n"
    "v_mfma_f32_16x16x32_bf16 v[48:51], a[240:243], a[0:3], v[48:51]\n"
    "v_cvt_pk_bf16_f32 v158, v184, v185\n"
    "v_exp_f32 v180, v124\n"
    "v_mfma_f32_16x16x32_bf16 v[52:55], a[240:243], a[4:7], v[52:55]\n"
    "v_add_f32 v218, v218, v184\n"
    "v_add_f32 v218, v218, v185\n"
    "ds_read_b64_tr_b16 a[224:225], v192 offset:16384\n"
    "v_mfma_f32_16x16x32_bf16 v[56:59], a[240:243], a[8:11], v[56:59]\n"
    "ds_read_b64_tr_b16 a[226:227], v192 offset:24576\n"
    "v_exp_f32 v181, v125\n"
    "v_cvt_pk_bf16_f32 v159, v186, v187\n"
    "v_mfma_f32_16x16x32_bf16 v[60:63], a[244:247], a[0:3], v[60:63]\n"
    "ds_read_b64_tr_b16 a[228:229], v193 offset:16384\n"
    "ds_read_b64_tr_b16 a[230:231], v193 offset:24576\n"
    "v_add_f32 v218, v218, v186\n"
    "v_mfma_f32_16x16x32_bf16 v[64:67], a[244:247], a[4:7], v[64:67]\n"
    "ds_read_b64_tr_b16 a[232:233], v194 offset:16384\n"
    "ds_read_b64_tr_b16 a[234:235], v194 offset:24576\n"
    "v_exp_f32 v182, v126\n"
    "ds_write_b128 v172, v[156:159] offset:12288\n"
    "v_mfma_f32_16x16x32_bf16 v[68:71], a[244:247], a[8:11], v[68:71]\n"
    "ds_read_b64_tr_b16 a[236:237], v195 offset:16384\n"
    "ds_read_b64_tr_b16 a[238:239], v195 offset:24576\n"
    "v_exp_f32 v183, v127\n"
    "v_add_f32 v218, v218, v187\n"
    "v_cvt_pk_bf16_f32 v160, v180, v181\n"
    "v_mfma_f32_16x16x32_bf16 v[72:75], a[248:251], a[0:3], v[72:75]\n"
    "v_exp_f32 v184, v136\n"
    "v_add_f32 v219, v180, v181\n"
    "v_mfma_f32_16x16x32_bf16 v[76:79], a[248:251], a[4:7], v[76:79]\n"
    "v_cvt_pk_bf16_f32 v161, v182, v183\n"
    "v_exp_f32 v185, v137\n"
    "v_mfma_f32_16x16x32_bf16 v[80:83], a[248:251], a[8:11], v[80:83]\n"
    "v_add_f32 v219, v219, v182\n"
    "v_exp_f32 v186, v138\n"
    "v_mfma_f32_16x16x32_bf16 v[84:87], a[252:255], a[0:3], v[84:87]\n"
    "v_add_f32 v219, v219, v183\n"
    "v_cvt_pk_bf16_f32 v162, v184, v185\n"
    "v_add_f32 v219, v219, v184\n"
    "v_mfma_f32_16x16x32_bf16 v[88:91], a[252:255], a[4:7], v[88:91]\n"
    "v_exp_f32 v187, v139\n"
    "v_add_f32 v219, v219, v185\n"
    "v_mfma_f32_16x16x32_bf16 v[92:95], a[252:255], a[8:11], v[92:95]\n"
    "v_exp_f32 v180, v128\n"
    "s_waitcnt lgkmcnt(0)\n"
    "v_mfma_f32_16x16x32_bf16 v[0:3], a[224:227], a[12:15], v[0:3]\n"
    "v_cvt_pk_bf16_f32 v163, v186, v187\n"
    "v_exp_f32 v181, v129\n"
    "v_mfma_f32_16x16x32_bf16 v[4:7], a[224:227], a[16:19], v[4:7]\n"
    "v_add_f32 v219, v219, v186\n"
    "ds_write_b128 v172, v[160:163] offset:13312\n"
    "ds_read_b64_tr_b16 a[240:241], v196 offset:16384\n"
    "v_mfma_f32_16x16x32_bf16 v[8:11], a[224:227], a[20:23], v[8:11]\n"
    "ds_read_b64_tr_b16 a[242:243], v196 offset:24576\n"
    "v_add_f32 v219, v219, v187\n"
    "ds_read_b64_tr_b16 a[244:245], v197 offset:16384\n"
    "v_mfma_f32_16x16x32_bf16 v[12:15], a[228:231], a[12:15], v[12:15]\n"
    "ds_read_b64_tr_b16 a[246:247], v197 offset:24576\n"
    "v_exp_f32 v182, v130\n"
    "v_cvt_pk_bf16_f32 v164, v180, v181\n"
    "v_mfma_f32_16x16x32_bf16 v[16:19], a[228:231], a[16:19], v[16:19]\n"
    "ds_read_b64_tr_b16 a[248:249], v198 offset:16384\n"
    "ds_read_b64_tr_b16 a[250:251], v198 offset:24576\n"
    "v_exp_f32 v183, v131\n"
    "v_add_f32 v220, v180, v181\n"
    "v_mfma_f32_16x16x32_bf16 v[20:23], a[228:231], a[20:23], v[20:23]\n"
    "ds_read_b64_tr_b16 a[252:253], v199 offset:16384\n"
    "ds_read_b64_tr_b16 a[254:255], v199 offset:24576\n"
    "v_exp_f32 v184, v140\n"
    "v_cvt_pk_bf16_f32 v165, v182, v183\n"
    "v_add_f32 v220, v220, v182\n"
    "v_mfma_f32_16x16x32_bf16 v[24:27], a[232:235], a[12:15], v[24:27]\n"
    "v_add_f32 v220, v220, v183\n"
    "v_exp_f32 v185, v141\n"
    "v_mfma_f32_16x16x32_bf16 v[28:31], a[232:235], a[16:19], v[28:31]\n"
    "v_exp_f32 v186, v142\n"
    "v_cvt_pk_bf16_f32 v166, v184, v185\n"
    "v_mfma_f32_16x16x32_bf16 v[32:35], a[232:235], a[20:23], v[32:35]\n"
    "v_add_f32 v220, v220, v184\n"
    "v_add_f32 v220, v220, v185\n"
    "v_mfma_f32_16x16x32_bf16 v[36:39], a[236:239], a[12:15], v[36:39]\n"
    "v_exp_f32 v187, v143\n"
    "s_nop 0\n"
    "v_cvt_pk_bf16_f32 v167, v186, v187\n"
    "v_mfma_f32_16x16x32_bf16 v[40:43], a[236:239], a[16:19], v[40:43]\n"
    "v_add_f32 v220, v220, v186\n"
    "ds_write_b128 v172, v[164:167] offset:14336\n"
    "v_add_f32 v220, v220, v187\n"
    "v_mfma_f32_16x16x32_bf16 v[44:47], a[236:239], a[20:23], v[44:47]\n"
    "v_max3_f32 v221, v218, v219, v220\n"
    "s_waitcnt lgkmcnt(1)\n"
    "v_mfma_f32_16x16x32_bf16 v[48:51], a[240:243], a[12:15], v[48:51]\n"
    "v_cmp_lt_f32 vcc, s70, v221\n"
    "s_cbranch_vccnz .Lfw4_%=\n"
    ".Lfb4_%=:\n"
    "v_subrev_u32 v192, 65536, v192\n"
    "v_mfma_f32_16x16x32_bf16 v[52:55], a[240:243], a[16:19], v[52:55]\n"
    "v_subrev_u32 v193, 65536, v193\n"
    "v_subrev_u32 v194, 65536, v194\n"
    "v_subrev_u32 v195, 65536, v195\n"
    "v_mfma_f32_16x16x32_bf16 v[56:59], a[240:243], a[20:23], v[56:59]\n"
    "v_subrev_u32 v196, 65536, v196\n"
    "v_subrev_u32 v197, 65536, v197\n"
    "v_subrev_u32 v198, 65536, v198\n"
    "v_mfma_f32_16x16x32_bf16 v[60:63], a[244:247], a[12:15], v[60:63]\n"
    "v_subrev_u32 v199, 65536, v199\n"
    "v_mfma_f32_16x16x32_bf16 v[64:67], a[244:247], a[16:19], v[64:67]\n"
    "s_waitcnt vmcnt(8) lgkmcnt(0)\n"
    "s_barrier\n"
    "v_mfma_f32_16x16x32_bf16 v[68:71], a[244:247], a[20:23], v[68:71]\n"
    "ds_read_b32 v168, v175 offset:0\n"
    "ds_read_b128 a[144:147], v173 offset:12288\n"
    "ds_read_b128 a[148:151], v173 offset:13312\n"
    "v_mfma_f32_16x16x32_bf16 v[72:75], a[248:251], a[12:15], v[72:75]\n"
    "ds_read_b128 a[152:155], v173 offset:14336\n"
    "ds_read_b128 a[156:159], v173 offset:18432\n"
    "ds_read_b128 a[160:163], v173 offset:19456\n"
    "v_mfma_f32_16x16x32_bf16 v[76:79], a[248:251], a[16:19], v[76:79]\n"
    "ds_read_b128 a[164:167], v173 offset:20480\n"
    "ds_read_b64_tr_b16 a[224:225], v192 offset:0\n"
    "ds_read_b64_tr_b16 a[226:227], v192 offset:8192\n"
    "v_mfma_f32_16x16x32_bf16 v[80:83], a[248:251], a[20:23], v[80:83]\n"
    "ds_read_b64_tr_b16 a[228:229], v192 offset:16384\n"
    "ds_read_b64_tr_b16 a[230:231], v192 offset:24576\n"
    "ds_read_b64_tr_b16 a[232:233], v193 offset:0\n"
    "v_mfma_f32_16x16x32_bf16 v[84:87], a[252:255], a[12:15], v[84:87]\n"
    "ds_read_b64_tr_b16 a[234:235], v193 offset:8192\n"
    "ds_read_b64_tr_b16 a[236:237], v193 offset:16384\n"
    "ds_read_b64_tr_b16 a[238:239], v193 offset:24576\n"
    "v_mfma_f32_16x16x32_bf16 v[88:91], a[252:255], a[16:19], v[88:91]\n"
    "v_mfma_f32_16x16x32_bf16 v[92:95], a[252:255], a[20:23], v[92:95]\n"
    "s_waitcnt lgkmcnt(14)\n"
    "v_cmp_ne_u32 vcc, 0, v168\n"
    "s_cbranch_vccnz .Ltr04_%=\n"
    ".Lbk04_%=:\n"
    "s_waitcnt lgkmcnt(4)\n"
    ".Lcommon_%=:\n"
    "v_mfma_f32_16x16x32_bf16 v[0:3], a[224:227], a[144:147], v[0:3]\n"
    "ds_read_b64_tr_b16 a[240:241], v194 offset:0\n"
    "ds_read_b64_tr_b16 a[242:243], v194 offset:8192\n"
    "ds_read_b64_tr_b16 a[244:245], v194 offset:16384\n"
    "ds_read_b64_tr_b16 a[246:247], v194 offset:24576\n"
    "v_mfma_f32_16x16x32_bf16 v[4:7], a[224:227], a[148:151], v[4:7]\n"
    "ds_read_b64_tr_b16 a[248:249], v195 offset:0\n"
    "ds_read_b64_tr_b16 a[250:251], v195 offset:8192\n"
    "v_mfma_f32_16x16x32_bf16 v[8:11], a[224:227], a[152:155], v[8:11]\n"
    "ds_read_b64_tr_b16 a[252:253], v195 offset:16384\n"
    "ds_read_b64_tr_b16 a[254:255], v195 offset:24576\n"
    "ds_read_b64_tr_b16 a[32:33], v196 offset:0\n"
    "ds_read_b64_tr_b16 a[34:35], v196 offset:8192\n"
    "v_mfma_f32_16x16x32_bf16 v[0:3], a[228:231], a[156:159], v[0:3]\n"
    "ds_read_b64_tr_b16 a[36:37], v196 offset:16384\n"
    "ds_read_b64_tr_b16 a[38:39], v196 offset:24576\n"
    "v_mfma_f32_16x16x32_bf16 v[4:7], a[228:231], a[160:163], v[4:7]\n"
    "ds_read_b64_tr_b16 a[40:41], v197 offset:0\n"
    "ds_read_b64_tr_b16 a[42:43], v197 offset:8192\n"
    "ds_read_b64_tr_b16 a[44:45], v197 offset:16384\n"
    "ds_read_b64_tr_b16 a[46:47], v197 offset:24576\n"
    "v_mfma_f32_16x16x32_bf16 v[8:11], a[228:231], a[164:167], v[8:11]\n"
    "ds_read_b64_tr_b16 a[48:49], v198 offset:0\n"
    "ds_read_b64_tr_b16 a[50:51], v198 offset:8192\n"
    "s_waitcnt lgkmcnt(15)\n"
    "v_mfma_f32_16x16x32_bf16 v[12:15], a[232:235], a[144:147], v[12:15]\n"
    "ds_read_b64_tr_b16 a[52:53], v198 offset:16384\n"
    "ds_read_b64_tr_b16 a[54:55], v198 offset:24576\n"
    "ds_read_b64_tr_b16 a[56:57], v199 offset:0\n"
    "ds_read_b64_tr_b16 a[58:59], v199 offset:8192\n"
    "v_mfma_f32_16x16x32_bf16 v[16:19], a[232:235], a[148:151], v[16:19]\n"
    "ds_read_b64_tr_b16 a[60:61], v199 offset:16384\n"
    "ds_read_b64_tr_b16 a[62:63], v199 offset:24576\n"
    "v_mfma_f32_16x16x32_bf16 v[20:23], a[232:235], a[152:155], v[20:23]\n"
    "v_xor_b32 v184, 65536, v223\n"
    "v_xor_b32 v185, 81920, v223\n"
    "v_mfma_f32_16x16x32_bf16 v[12:15], a[236:239], a[156:159], v[12:15]\n"
    "v_xor_b32 v186, 65568, v223\n"
    "v_xor_b32 v187, 81952, v223\n"
    "v_mfma_f32_16x16x32_bf16 v[16:19], a[236:239], a[160:163], v[16:19]\n"
    "s_add_u32 m0, s65, 0\n"
    "v_add_f32 v212, v212, v218\n"
    "v_mfma_f32_16x16x32_bf16 v[20:23], a[236:239], a[164:167], v[20:23]\n"
    "s_waitcnt vmcnt(0) lgkmcnt(0)\n"
    "s_barrier\n"
    "v_mfma_f32_16x16x32_bf16 v[24:27], a[240:243], a[144:147], v[24:27]\n"
    "ds_read_b64_tr_b16 a[192:193], v184 offset:32768\n"
    "ds_read_b64_tr_b16 a[194:195], v184 offset:34816\n"
    "v_mfma_f32_16x16x32_bf16 v[28:31], a[240:243], a[148:151], v[28:31]\n"
    "v_cvt_pk_bf16_f32 v0, v0, v1\n"
    "v_cvt_pk_bf16_f32 v1, v2, v3\n"
    "v_mfma_f32_16x16x32_bf16 v[32:35], a[240:243], a[152:155], v[32:35]\n"
    "ds_read_b64_tr_b16 a[196:197], v186 offset:32768\n"
    "ds_read_b64_tr_b16 a[198:199], v186 offset:34816\n"
    "buffer_load_dwordx4 v240, s[56:59], s66 offen offset:0 lds\n"
    "v_cvt_pk_bf16_f32 v2, v12, v13\n"
    "v_cvt_pk_bf16_f32 v3, v14, v15\n"
    "v_mfma_f32_16x16x32_bf16 v[24:27], a[244:247], a[156:159], v[24:27]\n"
    "v_add_f32 v213, v213, v219\n"
    "ds_read_b64_tr_b16 a[200:201], v184 offset:36864\n"
    "ds_read_b64_tr_b16 a[202:203], v184 offset:38912\n"
    "ds_write_b128 v249, v[0:3] offset:0\n"
    "v_mfma_f32_16x16x32_bf16 v[28:31], a[244:247], a[160:163], v[28:31]\n"
    "v_add_f32 v214, v214, v220\n"
    "ds_read_b64_tr_b16 a[204:205], v186 offset:36864\n"
    "ds_read_b64_tr_b16 a[206:207], v186 offset:38912\n"
    "v_cvt_pk_bf16_f32 v4, v4, v5\n"
    "v_cvt_pk_bf16_f32 v5, v6, v7\n"
    "v_mfma_f32_16x16x32_bf16 v[32:35], a[244:247], a[164:167], v[32:35]\n"
    "s_and_b32 s76, s64, 1\n"
    "ds_read_b64_tr_b16 a[208:209], v184 offset:40960\n"
    "ds_read_b64_tr_b16 a[210:211], v184 offset:43008\n"
    "v_cvt_pk_bf16_f32 v6, v16, v17\n"
    "v_cvt_pk_bf16_f32 v7, v18, v19\n"
    "v_mfma_f32_16x16x32_bf16 v[36:39], a[248:251], a[144:147], v[36:39]\n"
    "s_mul_i32 s76, s76, 1536\n"
    "ds_read_b64_tr_b16 a[212:213], v186 offset:40960\n"
    "ds_read_b64_tr_b16 a[214:215], v186 offset:43008\n"
    "buffer_load_dwordx4 v241, s[56:59], s66 offen offset:1024 lds\n"
    "ds_write_b128 v249, v[4:7] offset:1024\n"
    "v_mfma_f32_16x16x32_bf16 v[40:43], a[248:251], a[148:151], v[40:43]\n"
    "s_add_u32 s76, s76, 81920\n"
    "ds_read_b64_tr_b16 a[216:217], v184 offset:45056\n"
    "ds_read_b64_tr_b16 a[218:219], v184 offset:47104\n"
    "v_cvt_pk_bf16_f32 v8, v8, v9\n"
    "v_cvt_pk_bf16_f32 v9, v10, v11\n"
    "v_mfma_f32_16x16x32_bf16 v[44:47], a[248:251], a[152:155], v[44:47]\n"
    "v_add_u32 v252, s76, v253\n"
    "ds_read_b64_tr_b16 a[220:221], v186 offset:45056\n"
    "ds_read_b64_tr_b16 a[222:223], v186 offset:47104\n"
    "v_cvt_pk_bf16_f32 v10, v20, v21\n"
    "v_cvt_pk_bf16_f32 v11, v22, v23\n"
    "v_mfma_f32_16x16x32_bf16 v[36:39], a[252:255], a[156:159], v[36:39]\n"
    "s_lshl_b32 s77, s64, 7\n"
    "ds_write_b128 v249, v[8:11] offset:2048\n"
    "v_mfma_f32_16x16x32_bf16 v[40:43], a[252:255], a[160:163], v[40:43]\n"
    "s_and_b32 s77, s77, 256\n"
    "buffer_load_dwordx4 v242, s[56:59], s66 offen offset:2048 lds\n"
    "v_mfma_f32_16x16x32_bf16 v[44:47], a[252:255], a[164:167], v[44:47]\n"
    "s_add_u32 s76, s76, s77\n"
    "v_lshl_add_u32 v251, v254, 2, s76\n"
    "v_mfma_f32_16x16x32_bf16 v[48:51], a[32:35], a[144:147], v[48:51]\n"
    "ds_write_b32 v251, v212 offset:0\n"
    "ds_write_b32 v251, v213 offset:512\n"
    "v_mfma_f32_16x16x32_bf16 v[52:55], a[32:35], a[148:151], v[52:55]\n"
    "v_cvt_pk_bf16_f32 v24, v24, v25\n"
    "v_cvt_pk_bf16_f32 v25, v26, v27\n"
    "v_mfma_f32_16x16x32_bf16 v[56:59], a[32:35], a[152:155], v[56:59]\n"
    "buffer_load_dwordx4 v243, s[56:59], s66 offen offset:3072 lds\n"
    "v_cvt_pk_bf16_f32 v26, v36, v37\n"
    "v_cvt_pk_bf16_f32 v27, v38, v39\n"
    "v_mfma_f32_16x16x32_bf16 v[48:51], a[36:39], a[156:159], v[48:51]\n"
    "ds_write_b32 v251, v214 offset:1024\n"
    "ds_write_b128 v249, v[24:27] offset:3072\n"
    "v_mfma_f32_16x16x32_bf16 v[52:55], a[36:39], a[160:163], v[52:55]\n"
    "v_cvt_pk_bf16_f32 v28, v28, v29\n"
    "v_cvt_pk_bf16_f32 v29, v30, v31\n"
    "v_mfma_f32_16x16x32_bf16 v[56:59], a[36:39], a[164:167], v[56:59]\n"
    "s_add_u32 m0, s65, 4096\n"
    "v_cvt_pk_bf16_f32 v30, v40, v41\n"
    "v_cvt_pk_bf16_f32 v31, v42, v43\n"
    "v_mfma_f32_16x16x32_bf16 v[60:63], a[40:43], a[144:147], v[60:63]\n"
    "buffer_load_dwordx4 v244, s[56:59], s66 offen offset:0 lds\n"
    "ds_write_b128 v249, v[28:31] offset:4096\n"
    "v_mfma_f32_16x16x32_bf16 v[64:67], a[40:43], a[148:151], v[64:67]\n"
    "v_cvt_pk_bf16_f32 v32, v32, v33\n"
    "v_cvt_pk_bf16_f32 v33, v34, v35\n"
    "v_mfma_f32_16x16x32_bf16 v[68:71], a[40:43], a[152:155], v[68:71]\n"
    "v_cvt_pk_bf16_f32 v34, v44, v45\n"
    "v_cvt_pk_bf16_f32 v35, v46, v47\n"
    "v_mfma_f32_16x16x32_bf16 v[60:63], a[44:47], a[156:159], v[60:63]\n"
    "ds_write_b128 v249, v[32:35] offset:5120\n"
    "buffer_load_dwordx4 v245, s[56:59], s66 offen offset:1024 lds\n"
    "v_mfma_f32_16x16x32_bf16 v[64:67], a[44:47], a[160:163], v[64:67]\n"
    "v_add_u32 v232, 1024, v252\n"
    "v_mfma_f32_16x16x32_bf16 v[68:71], a[44:47], a[164:167], v[68:71]\n"
    "v_mfma_f32_16x16x32_bf16 v[72:75], a[48:51], a[144:147], v[72:75]\n"
    "v_mfma_f32_16x16x32_bf16 v[76:79], a[48:51], a[148:151], v[76:79]\n"
    "v_cvt_pk_bf16_f32 v48, v48, v49\n"
    "v_cvt_pk_bf16_f32 v49, v50, v51\n"
    "v_mfma_f32_16x16x32_bf16 v[80:83], a[48:51], a[152:155], v[80:83]\n"
    "buffer_load_dwordx4 v246, s[56:59], s66 offen offset:2048 lds\n"
    "v_cvt_pk_bf16_f32 v50, v60, v61\n"
    "v_cvt_pk_bf16_f32 v51, v62, v63\n"
    "v_mfma_f32_16x16x32_bf16 v[72:75], a[52:55], a[156:159], v[72:75]\n"
    "ds_write_b128 v249, v[48:51] offset:6144\n"
    "v_cvt_pk_bf16_f32 v52, v52, v53\n"
    "v_mfma_f32_16x16x32_bf16 v[76:79], a[52:55], a[160:163], v[76:79]\n"
    "v_cvt_pk_bf16_f32 v53, v54, v55\n"
    "v_cvt_pk_bf16_f32 v54, v64, v65\n"
    "v_mfma_f32_16x16x32_bf16 v[80:83], a[52:55], a[164:167], v[80:83]\n"
    "v_cvt_pk_bf16_f32 v55, v66, v67\n"
    "buffer_load_dwordx4 v247, s[56:59], s66 offen offset:3072 lds\n"
    "v_mfma_f32_16x16x32_bf16 v[84:87], a[56:59], a[144:147], v[84:87]\n"
    "ds_write_b128 v249, v[52:55] offset:7168\n"
    "v_cvt_pk_bf16_f32 v56, v56, v57\n"
    "v_mfma_f32_16x16x32_bf16 v[88:91], a[56:59], a[148:151], v[88:91]\n"
    "v_cvt_pk_bf16_f32 v57, v58, v59\n"
    "v_cvt_pk_bf16_f32 v58, v68, v69\n"
    "v_mfma_f32_16x16x32_bf16 v[92:95], a[56:59], a[152:155], v[92:95]\n"
    "v_cvt_pk_bf16_f32 v59, v70, v71\n"
    "s_add_u32 s66, s66, 128\n"
    "v_mfma_f32_16x16x32_bf16 v[84:87], a[60:63], a[156:159], v[84:87]\n"
    "ds_write_b128 v249, v[56:59] offset:8192\n"
    "v_mfma_f32_16x16x32_bf16 v[88:91], a[60:63], a[160:163], v[88:91]\n"
    "v_mfma_f32_16x16x32_bf16 v[92:95], a[60:63], a[164:167], v[92:95]\n"
    "s_waitcnt lgkmcnt(15)\n"
    "v_mfma_f32_16x16x32_bf16 v[96:99], a[192:195], v[0:3], 0\n"
    "v_mfma_f32_16x16x32_bf16 v[104:107], a[192:195], v[4:7], 0\n"
    "v_cvt_pk_bf16_f32 v72, v72, v73\n"
    "v_cvt_pk_bf16_f32 v73, v74, v75\n"
    "v_mfma_f32_16x16x32_bf16 v[112:115], a[192:195], v[8:11], 0\n"
    "v_cvt_pk_bf16_f32 v74, v84, v85\n"
    "v_cvt_pk_bf16_f32 v75, v86, v87\n"
    "v_mfma_f32_16x16x32_bf16 v[100:103], a[196:199], v[0:3], 0\n"
    "ds_write_b128 v249, v[72:75] offset:9216\n"
    "v_cvt_pk_bf16_f32 v76, v76, v77\n"
    "v_mfma_f32_16x16x32_bf16 v[108:111], a[196:199], v[4:7], 0\n"
    "v_cvt_pk_bf16_f32 v77, v78, v79\n"
    "v_cvt_pk_bf16_f32 v78, v88, v89\n"
    "v_mfma_f32_16x16x32_bf16 v[116:119], a[196:199], v[8:11], 0\n"
    "v_cvt_pk_bf16_f32 v79, v90, v91\n"
    "v_mfma_f32_16x16x32_bf16 v[96:99], a[200:203], v[24:27], v[96:99]\n"
    "ds_write_b128 v249, v[76:79] offset:10240\n"
    "v_cvt_pk_bf16_f32 v80, v80, v81\n"
    "v_mfma_f32_16x16x32_bf16 v[104:107], a[200:203], v[28:31], v[104:107]\n"
    "v_cvt_pk_bf16_f32 v81, v82, v83\n"
    "v_cvt_pk_bf16_f32 v82, v92, v93\n"
    "v_mfma_f32_16x16x32_bf16 v[112:115], a[200:203], v[32:35], v[112:115]\n"
    "v_cvt_pk_bf16_f32 v83, v94, v95\n"
    "ds_write_b128 v249, v[80:83] offset:11264\n"
    "v_mfma_f32_16x16x32_bf16 v[100:103], a[204:207], v[24:27], v[100:103]\n"
    "v_mfma_f32_16x16x32_bf16 v[108:111], a[204:207], v[28:31], v[108:111]\n"
    "v_mfma_f32_16x16x32_bf16 v[116:119], a[204:207], v[32:35], v[116:119]\n"
    "v_mfma_f32_16x16x32_bf16 v[96:99], a[208:211], v[48:51], v[96:99]\n"
    "s_waitcnt lgkmcnt(0)\n"
    "s_barrier\n"
    "v_mfma_f32_16x16x32_bf16 v[104:107], a[208:211], v[52:55], v[104:107]\n"
    "ds_read_b64_tr_b16 a[64:65], v185 offset:32768\n"
    "ds_read_b64_tr_b16 a[66:67], v185 offset:34816\n"
    "ds_read_b64_tr_b16 a[68:69], v187 offset:32768\n"
    "ds_read_b64_tr_b16 a[70:71], v187 offset:34816\n"
    "ds_read_b64_tr_b16 a[72:73], v185 offset:36864\n"
    "ds_read_b64_tr_b16 a[74:75], v185 offset:38912\n"
    "ds_read_b64_tr_b16 a[76:77], v187 offset:36864\n"
    "ds_read_b64_tr_b16 a[78:79], v187 offset:38912\n"
    "ds_read_b64_tr_b16 a[80:81], v185 offset:40960\n"
    "ds_read_b64_tr_b16 a[82:83], v185 offset:43008\n"
    "ds_read_b64_tr_b16 a[84:85], v187 offset:40960\n"
    "ds_read_b64_tr_b16 a[86:87], v187 offset:43008\n"
    "ds_read_b64_tr_b16 a[88:89], v185 offset:45056\n"
    "ds_read_b64_tr_b16 a[90:91], v185 offset:47104\n"
    "ds_read_b64_tr_b16 a[92:93], v187 offset:45056\n"
    "ds_read_b64_tr_b16 a[94:95], v187 offset:47104\n"
    "ds_read_b128 a[96:99], v248 offset:0\n"
    "ds_read_b128 a[100:103], v248 offset:1024\n"
    "ds_read_b128 a[104:107], v248 offset:2048\n"
    "v_mfma_f32_16x16x32_bf16 v[112:115], a[208:211], v[56:59], v[112:115]\n"
    "ds_read_b128 a[108:111], v248 offset:3072\n"
    "ds_read_b128 a[112:115], v248 offset:4096\n"
    "ds_read_b128 a[116:119], v248 offset:5120\n"
    "v_mfma_f32_16x16x32_bf16 v[100:103], a[212:215], v[48:51], v[100:103]\n"
    "ds_read_b128 a[120:123], v248 offset:6144\n"
    "ds_read_b128 a[124:127], v248 offset:7168\n"
    "ds_read_b128 a[128:131], v248 offset:8192\n"
    "v_mfma_f32_16x16x32_bf16 v[108:111], a[212:215], v[52:55], v[108:111]\n"
    "s_add_u32 m0, s65, 131072\n"
    "ds_read_b128 a[132:135], v248 offset:9216\n"
    "ds_read_b128 a[136:139], v248 offset:10240\n"
    "ds_read_b128 a[140:143], v248 offset:11264\n"
    "v_mfma_f32_16x16x32_bf16 v[116:119], a[212:215], v[56:59], v[116:119]\n"
    "buffer_load_dwordx4 v240, s[56:59], s66 offen offset:0 lds\n"
    "v_mfma_f32_16x16x32_bf16 v[96:99], a[216:219], v[72:75], v[96:99]\n"
    "v_xor_b32 v180, 65536, v184\n"
    "v_xor_b32 v181, 65536, v185\n"
    "v_mfma_f32_16x16x32_bf16 v[104:107], a[216:219], v[76:79], v[104:107]\n"
    "v_xor_b32 v182, 65536, v186\n"
    "v_xor_b32 v183, 65536, v187\n"
    "v_mfma_f32_16x16x32_bf16 v[112:115], a[216:219], v[80:83], v[112:115]\n"
    "buffer_load_dwordx4 v241, s[56:59], s66 offen offset:1024 lds\n"
    "ds_read2_b32 v[120:121], v252 offset0:0 offset1:16\n"
    "v_mfma_f32_16x16x32_bf16 v[100:103], a[220:223], v[72:75], v[100:103]\n"
    "ds_read2_b32 v[122:123], v252 offset0:32 offset1:48\n"
    "ds_read2_b32 v[124:125], v252 offset0:64 offset1:80\n"
    "v_mfma_f32_16x16x32_bf16 v[108:111], a[220:223], v[76:79], v[108:111]\n"
    "ds_read2_b32 v[126:127], v252 offset0:96 offset1:112\n"
    "ds_read2_b32 v[128:129], v252 offset0:128 offset1:144\n"
    "v_mfma_f32_16x16x32_bf16 v[116:119], a[220:223], v[80:83], v[116:119]\n"
    "ds_read2_b32 v[130:131], v252 offset0:160 offset1:176\n"
    "s_waitcnt lgkmcnt(15)\n"
    "v_mfma_f32_16x16x32_bf16 v[96:99], a[64:67], a[96:99], v[96:99]\n"
    "ds_read2_b32 v[132:133], v252 offset0:192 offset1:208\n"
    "ds_read2_b32 v[134:135], v252 offset0:224 offset1:240\n"
    "v_mfma_f32_16x16x32_bf16 v[104:107], a[64:67], a[100:103], v[104:107]\n"
    "buffer_load_dwordx4 v242, s[56:59], s66 offen offset:2048 lds\n"
    "ds_read2_b32 v[136:137], v232 offset0:0 offset1:16\n"
    "ds_read2_b32 v[138:139], v232 offset0:32 offset1:48\n"
    "ds_read2_b32 v[140:141], v232 offset0:64 offset1:80\n"
    "ds_read2_b32 v[142:143], v232 offset0:96 offset1:112\n"
    "v_mfma_f32_16x16x32_bf16 v[112:115], a[64:67], a[104:107], v[112:115]\n"
    "v_add_u32 v188, 131072, v180\n"
    "v_add_u32 v189, 131072, v181\n"
    "v_mfma_f32_16x16x32_bf16 v[100:103], a[68:71], a[96:99], v[100:103]\n"
    "v_add_u32 v190, 131072, v182\n"
    "v_add_u32 v191, 131072, v183\n"
    "v_mfma_f32_16x16x32_bf16 v[108:111], a[68:71], a[100:103], v[108:111]\n"
    "buffer_load_dwordx4 v243, s[56:59], s66 offen offset:3072 lds\n"
    "s_add_u32 m0, s65, 135168\n"
    "v_mfma_f32_16x16x32_bf16 v[116:119], a[68:71], a[104:107], v[116:119]\n"
    "s_waitcnt lgkmcnt(0)\n"
    "v_mfma_f32_16x16x32_bf16 v[96:99], a[72:75], a[108:111], v[96:99]\n"
    "v_add_f32 v120, v120, v121\n"
    "v_add_f32 v122, v122, v123\n"
    "v_mfma_f32_16x16x32_bf16 v[104:107], a[72:75], a[112:115], v[104:107]\n"
    "v_add_f32 v124, v124, v125\n"
    "v_add_f32 v126, v126, v127\n"
    "v_mfma_f32_16x16x32_bf16 v[112:115], a[72:75], a[116:119], v[112:115]\n"
    "buffer_load_dwordx4 v244, s[56:59], s66 offen offset:0 lds\n"
    "v_add_f32 v120, v120, v122\n"
    "v_mfma_f32_16x16x32_bf16 v[100:103], a[76:79], a[108:111], v[100:103]\n"
    "v_add_f32 v124, v124, v126\n"
    "v_add_f32 v162, v120, v124\n"
    "v_mfma_f32_16x16x32_bf16 v[108:111], a[76:79], a[112:115], v[108:111]\n"
    "v_add_f32 v128, v128, v129\n"
    "v_add_f32 v130, v130, v131\n"
    "v_mfma_f32_16x16x32_bf16 v[116:119], a[76:79], a[116:119], v[116:119]\n"
    "v_add_f32 v132, v132, v133\n"
    "v_mfma_f32_16x16x32_bf16 v[96:99], a[80:83], a[120:123], v[96:99]\n"
    "buffer_load_dwordx4 v245, s[56:59], s66 offen offset:1024 lds\n"
    "v_add_f32 v134, v134, v135\n"
    "v_mfma_f32_16x16x32_bf16 v[104:107], a[80:83], a[124:127], v[104:107]\n"
    "v_add_f32 v128, v128, v130\n"
    "v_add_f32 v132, v132, v134\n"
    "v_mfma_f32_16x16x32_bf16 v[112:115], a[80:83], a[128:131], v[112:115]\n"
    "v_add_f32 v163, v128, v132\n"
    "v_add_f32 v136, v136, v137\n"
    "v_mfma_f32_16x16x32_bf16 v[100:103], a[84:87], a[120:123], v[100:103]\n"
    "v_add_f32 v138, v138, v139\n"
    "v_add_f32 v140, v140, v141\n"
    "v_mfma_f32_16x16x32_bf16 v[108:111], a[84:87], a[124:127], v[108:111]\n"
    "s_waitcnt vmcnt(6)\n"
    "s_barrier\n"
    "v_mfma_f32_16x16x32_bf16 v[116:119], a[84:87], a[128:131], v[116:119]\n"
    "buffer_load_dwordx4 v246, s[56:59], s66 offen offset:2048 lds\n"
    "ds_read_b64_tr_b16 a[192:193], v180 offset:0\n"
    "ds_read_b64_tr_b16 a[194:195], v180 offset:2048\n"
    "v_mfma_f32_16x16x32_bf16 v[96:99], a[88:91], a[132:135], v[96:99]\n"
    "ds_read_b64_tr_b16 a[196:197], v182 offset:0\n"
    "ds_read_b64_tr_b16 a[198:199], v182 offset:2048\n"
    "v_add_f32 v142, v142, v143\n"
    "v_mfma_f32_16x16x32_bf16 v[104:107], a[88:91], a[136:139], v[104:107]\n"
    "v_add_f32 v136, v136, v138\n"
    "v_add_f32 v140, v140, v142\n"
    "v_mfma_f32_16x16x32_bf16 v[112:115], a[88:91], a[140:143], v[112:115]\n"
    "ds_read_b64_tr_b16 a[200:201], v180 offset:4096\n"
    "ds_read_b64_tr_b16 a[202:203], v180 offset:6144\n"
    "v_add_f32 v164, v136, v140\n"
    "v_mfma_f32_16x16x32_bf16 v[100:103], a[92:95], a[132:135], v[100:103]\n"
    "buffer_load_dwordx4 v247, s[56:59], s66 offen offset:3072 lds\n"
    "ds_read_b64_tr_b16 a[204:205], v182 offset:4096\n"
    "ds_read_b64_tr_b16 a[206:207], v182 offset:6144\n"
    "v_rcp_f32 v162, v162\n"
    "v_rcp_f32 v163, v163\n"
    "v_mfma_f32_16x16x32_bf16 v[108:111], a[92:95], a[136:139], v[108:111]\n"
    "s_add_u32 s66, s66, 128\n"
    "v_rcp_f32 v164, v164\n"
    "v_mfma_f32_16x16x32_bf16 v[116:119], a[92:95], a[140:143], v[116:119]\n"
    "s_waitcnt lgkmcnt(4)\n"
    "v_mfma_f32_16x16x32_bf16 v[120:123], a[192:195], v[0:3], 0\n"
    "s_add_u32 m0, s65, 98304\n"
    "ds_read_b64_tr_b16 a[208:209], v180 offset:8192\n"
    "v_mfma_f32_16x16x32_bf16 v[128:131], a[192:195], v[4:7], 0\n"
    "ds_read_b64_tr_b16 a[210:211], v180 offset:10240\n"
    "buffer_load_dwordx4 v240, s[56:59], s66 offen offset:0 lds\n"
    "v_mfma_f32_16x16x32_bf16 v[136:139], a[192:195], v[8:11], 0\n"
    "ds_read_b64_tr_b16 a[212:213], v182 offset:8192\n"
    "ds_read_b64_tr_b16 a[214:215], v182 offset:10240\n"
    "v_mfma_f32_16x16x32_bf16 v[124:127], a[196:199], v[0:3], 0\n"
    "v_mul_f32 v96, v96, v162\n"
    "v_mul_f32 v97, v97, v162\n"
    "v_mfma_f32_16x16x32_bf16 v[132:135], a[196:199], v[4:7], 0\n"
    "buffer_load_dwordx4 v241, s[56:59], s66 offen offset:1024 lds\n"
    "v_mul_f32 v98, v98, v162\n"
    "v_mfma_f32_16x16x32_bf16 v[140:143], a[196:199], v[8:11], 0\n"
    "v_mul_f32 v99, v99, v162\n"
    "s_waitcnt lgkmcnt(4)\n"
    "v_mfma_f32_16x16x32_bf16 v[120:123], a[200:203], v[24:27], v[120:123]\n"
    "v_mul_f32 v100, v100, v162\n"
    "ds_read_b64_tr_b16 a[216:217], v180 offset:12288\n"
    "v_mfma_f32_16x16x32_bf16 v[128:131], a[200:203], v[28:31], v[128:131]\n"
    "ds_read_b64_tr_b16 a[218:219], v180 offset:14336\n"
    "buffer_load_dwordx4 v242, s[56:59], s66 offen offset:2048 lds\n"
    "v_mfma_f32_16x16x32_bf16 v[136:139], a[200:203], v[32:35], v[136:139]\n"
    "v_mul_f32 v101, v101, v162\n"
    "ds_read_b64_tr_b16 a[220:221], v182 offset:12288\n"
    "v_mfma_f32_16x16x32_bf16 v[124:127], a[204:207], v[24:27], v[124:127]\n"
    "ds_read_b64_tr_b16 a[222:223], v182 offset:14336\n"
    "v_mul_f32 v102, v102, v162\n"
    "v_mfma_f32_16x16x32_bf16 v[132:135], a[204:207], v[28:31], v[132:135]\n"
    "v_mul_f32 v103, v103, v162\n"
    "v_cvt_pk_bf16_f32 v96, v96, v97\n"
    "v_mfma_f32_16x16x32_bf16 v[140:143], a[204:207], v[32:35], v[140:143]\n"
    "buffer_load_dwordx4 v243, s[56:59], s66 offen offset:3072 lds\n"
    "v_cvt_pk_bf16_f32 v97, v98, v99\n"
    "s_waitcnt lgkmcnt(4)\n"
    "v_mfma_f32_16x16x32_bf16 v[120:123], a[208:211], v[48:51], v[120:123]\n"
    "v_cvt_pk_bf16_f32 v98, v100, v101\n"
    "s_add_u32 m0, s65, 102400\n"
    "v_mfma_f32_16x16x32_bf16 v[128:131], a[208:211], v[52:55], v[128:131]\n"
    "v_cvt_pk_bf16_f32 v99, v102, v103\n"
    "ds_read_b64_tr_b16 a[192:193], v181 offset:0\n"
    "v_mfma_f32_16x16x32_bf16 v[136:139], a[208:211], v[56:59], v[136:139]\n"
    "ds_read_b64_tr_b16 a[194:195], v181 offset:2048\n"
    "buffer_load_dwordx4 v244, s[56:59], s66 offen offset:0 lds\n"
    "s_nop 0\n"
    "v_mfma_f32_16x16x32_bf16 v[124:127], a[212:215], v[48:51], v[124:127]\n"
    "ds_read_b64_tr_b16 a[196:197], v183 offset:0\n"
    "ds_read_b64_tr_b16 a[198:199], v183 offset:2048\n"
    "v_permlane16_swap_b32 v96, v98\n"
    "v_mfma_f32_16x16x32_bf16 v[132:135], a[212:215], v[52:55], v[132:135]\n"
    "v_permlane16_swap_b32 v97, v99\n"
    "buffer_store_dwordx4 v[96:99], v250, s[60:63], s71 offen offset:0 sc1\n"
    "v_mfma_f32_16x16x32_bf16 v[140:143], a[212:215], v[56:59], v[140:143]\n"
    "buffer_load_dwordx4 v245, s[56:59], s66 offen offset:1024 lds\n"
    "v_mul_f32 v104, v104, v163\n"
    "s_waitcnt lgkmcnt(4)\n"
    "v_mfma_f32_16x16x32_bf16 v[120:123], a[216:219], v[72:75], v[120:123]\n"
    "v_mul_f32 v105, v105, v163\n"
    "v_mul_f32 v106, v106, v163\n"
    "v_mfma_f32_16x16x32_bf16 v[128:131], a[216:219], v[76:79], v[128:131]\n"
    "ds_read_b64_tr_b16 a[200:201], v181 offset:4096\n"
    "ds_read_b64_tr_b16 a[202:203], v181 offset:6144\n"
    "v_mfma_f32_16x16x32_bf16 v[136:139], a[216:219], v[80:83], v[136:139]\n"
    "buffer_load_dwordx4 v246, s[56:59], s66 offen offset:2048 lds\n"
    "v_mul_f32 v107, v107, v163\n"
    "v_mfma_f32_16x16x32_bf16 v[124:127], a[220:223], v[72:75], v[124:127]\n"
    "ds_read_b64_tr_b16 a[204:205], v183 offset:4096\n"
    "ds_read_b64_tr_b16 a[206:207], v183 offset:6144\n"
    "v_mul_f32 v108, v108, v163\n"
    "v_mfma_f32_16x16x32_bf16 v[132:135], a[220:223], v[76:79], v[132:135]\n"
    "v_mul_f32 v109, v109, v163\n"
    "v_mul_f32 v110, v110, v163\n"
    "v_mfma_f32_16x16x32_bf16 v[140:143], a[220:223], v[80:83], v[140:143]\n"
    "buffer_load_dwordx4 v247, s[56:59], s66 offen offset:3072 lds\n"
    "v_mul_f32 v111, v111, v163\n"
    "s_waitcnt lgkmcnt(4)\n"
    "v_mfma_f32_16x16x32_bf16 v[120:123], a[192:195], a[96:99], v[120:123]\n"
    "v_cvt_pk_bf16_f32 v104, v104, v105\n"
    "s_add_u32 s66, s66, 128\n"
    "v_mfma_f32_16x16x32_bf16 v[128:131], a[192:195], a[100:103], v[128:131]\n"
    "v_cvt_pk_bf16_f32 v105, v106, v107\n"
    "ds_read_b64_tr_b16 a[208:209], v181 offset:8192\n"
    "v_mfma_f32_16x16x32_bf16 v[136:139], a[192:195], a[104:107], v[136:139]\n"
    "ds_read_b64_tr_b16 a[210:211], v181 offset:10240\n"
    "v_cvt_pk_bf16_f32 v106, v108, v109\n"
    "v_mfma_f32_16x16x32_bf16 v[124:127], a[196:199], a[96:99], v[124:127]\n"
    "ds_read_b64_tr_b16 a[212:213], v183 offset:8192\n"
    "ds_read_b64_tr_b16 a[214:215], v183 offset:10240\n"
    "v_cvt_pk_bf16_f32 v107, v110, v111\n"
    "v_mfma_f32_16x16x32_bf16 v[132:135], a[196:199], a[100:103], v[132:135]\n"
    "s_nop 0\n"
    "v_permlane16_swap_b32 v104, v106\n"
    "v_mfma_f32_16x16x32_bf16 v[140:143], a[196:199], a[104:107], v[140:143]\n"
    "v_permlane16_swap_b32 v105, v107\n"
    "s_waitcnt lgkmcnt(4)\n"
    "v_mfma_f32_16x16x32_bf16 v[120:123], a[200:203], a[108:111], v[120:123]\n"
    "buffer_store_dwordx4 v[104:107], v250, s[60:63], s71 offen offset:1024 sc1\n"
    "v_mul_f32 v112, v112, v164\n"
    "v_mfma_f32_16x16x32_bf16 v[128:131], a[200:203], a[112:115], v[128:131]\n"
    "ds_read_b64_tr_b16 a[216:217], v181 offset:12288\n"
    "ds_read_b64_tr_b16 a[218:219], v181 offset:14336\n"
    "v_mfma_f32_16x16x32_bf16 v[136:139], a[200:203], a[116:119], v[136:139]\n"
    "v_mul_f32 v113, v113, v164\n"
    "ds_read_b64_tr_b16 a[220:221], v183 offset:12288\n"
    "v_mfma_f32_16x16x32_bf16 v[124:127], a[204:207], a[108:111], v[124:127]\n"
    "ds_read_b64_tr_b16 a[222:223], v183 offset:14336\n"
    "v_mul_f32 v114, v114, v164\n"
    "v_mfma_f32_16x16x32_bf16 v[132:135], a[204:207], a[112:115], v[132:135]\n"
    "v_mul_f32 v115, v115, v164\n"
    "v_mul_f32 v116, v116, v164\n"
    "v_mfma_f32_16x16x32_bf16 v[140:143], a[204:207], a[116:119], v[140:143]\n"
    "v_mul_f32 v117, v117, v164\n"
    "s_waitcnt lgkmcnt(4)\n"
    "v_mfma_f32_16x16x32_bf16 v[120:123], a[208:211], a[120:123], v[120:123]\n"
    "v_mul_f32 v118, v118, v164\n"
    "v_mul_f32 v119, v119, v164\n"
    "v_mfma_f32_16x16x32_bf16 v[128:131], a[208:211], a[124:127], v[128:131]\n"
    "v_cvt_pk_bf16_f32 v112, v112, v113\n"
    "v_cvt_pk_bf16_f32 v113, v114, v115\n"
    "v_mfma_f32_16x16x32_bf16 v[136:139], a[208:211], a[128:131], v[136:139]\n"
    "v_cvt_pk_bf16_f32 v114, v116, v117\n"
    "v_cvt_pk_bf16_f32 v115, v118, v119\n"
    "v_mfma_f32_16x16x32_bf16 v[124:127], a[212:215], a[120:123], v[124:127]\n"
    "s_nop 0\n"
    "v_permlane16_swap_b32 v112, v114\n"
    "v_mfma_f32_16x16x32_bf16 v[132:135], a[212:215], a[124:127], v[132:135]\n"
    "s_waitcnt vmcnt(10) lgkmcnt(0)\n"
    "s_barrier\n"
    "v_mfma_f32_16x16x32_bf16 v[140:143], a[212:215], a[128:131], v[140:143]\n"
    "ds_read_b64_tr_b16 a[192:193], v188 offset:0\n"
    "ds_read_b64_tr_b16 a[194:195], v188 offset:2048\n"
    "v_mfma_f32_16x16x32_bf16 v[120:123], a[216:219], a[132:135], v[120:123]\n"
    "ds_read_b64_tr_b16 a[196:197], v190 offset:0\n"
    "ds_read_b64_tr_b16 a[198:199], v190 offset:2048\n"
    "v_mfma_f32_16x16x32_bf16 v[128:131], a[216:219], a[136:139], v[128:131]\n"
    "v_permlane16_swap_b32 v113, v115\n"
    "ds_read_b64_tr_b16 a[200:201], v188 offset:4096\n"
    "v_mfma_f32_16x16x32_bf16 v[136:139], a[216:219], a[140:143], v[136:139]\n"
    "ds_read_b64_tr_b16 a[202:203], v188 offset:6144\n"
    "buffer_store_dwordx4 v[112:115], v250, s[60:63], s71 offen offset:2048 sc1\n"
    "v_mfma_f32_16x16x32_bf16 v[124:127], a[220:223], a[132:135], v[124:127]\n"
    "ds_read_b64_tr_b16 a[204:205], v190 offset:4096\n"
    "ds_read_b64_tr_b16 a[206:207], v190 offset:6144\n"
    "s_add_u32 s71, s71, 12288\n"
    "v_mfma_f32_16x16x32_bf16 v[132:135], a[220:223], a[136:139], v[132:135]\n"
    "v_mfma_f32_16x16x32_bf16 v[140:143], a[220:223], a[140:143], v[140:143]\n"
    "s_waitcnt lgkmcnt(4)\n"
    "v_mfma_f32_16x16x32_bf16 v[96:99], a[192:195], v[0:3], 0\n"
    "s_add_u32 m0, s65, 32768\n"
    "ds_read_b64_tr_b16 a[208:209], v188 offset:8192\n"
    "v_mfma_f32_16x16x32_bf16 v[104:107], a[192:195], v[4:7], 0\n"
    "ds_read_b64_tr_b16 a[210:211], v188 offset:10240\n"
    "buffer_load_dwordx4 v240, s[56:59], s66 offen offset:0 lds\n"
    "v_mfma_f32_16x16x32_bf16 v[112:115], a[192:195], v[8:11], 0\n"
    "ds_read_b64_tr_b16 a[212:213], v190 offset:8192\n"
    "ds_read_b64_tr_b16 a[214:215], v190 offset:10240\n"
    "v_mfma_f32_16x16x32_bf16 v[100:103], a[196:199], v[0:3], 0\n"
    "v_mul_f32 v120, v120, v162\n"
    "v_mul_f32 v121, v121, v162\n"
    "v_mfma_f32_16x16x32_bf16 v[108:111], a[196:199], v[4:7], 0\n"
    "buffer_load_dwordx4 v241, s[56:59], s66 offen offset:1024 lds\n"
    "v_mul_f32 v122, v122, v162\n"
    "v_mfma_f32_16x16x32_bf16 v[116:119], a[196:199], v[8:11], 0\n"
    "v_mul_f32 v123, v123, v162\n"
    "s_waitcnt lgkmcnt(4)\n"
    "v_mfma_f32_16x16x32_bf16 v[96:99], a[200:203], v[24:27], v[96:99]\n"
    "v_mul_f32 v124, v124, v162\n"
    "ds_read_b64_tr_b16 a[216:217], v188 offset:12288\n"
    "v_mfma_f32_16x16x32_bf16 v[104:107], a[200:203], v[28:31], v[104:107]\n"
    "ds_read_b64_tr_b16 a[218:219], v188 offset:14336\n"
    "buffer_load_dwordx4 v242, s[56:59], s66 offen offset:2048 lds\n"
    "v_mfma_f32_16x16x32_bf16 v[112:115], a[200:203], v[32:35], v[112:115]\n"
    "v_mul_f32 v125, v125, v162\n"
    "ds_read_b64_tr_b16 a[220:221], v190 offset:12288\n"
    "v_mfma_f32_16x16x32_bf16 v[100:103], a[204:207], v[24:27], v[100:103]\n"
    "ds_read_b64_tr_b16 a[222:223], v190 offset:14336\n"
    "v_mul_f32 v126, v126, v162\n"
    "v_mfma_f32_16x16x32_bf16 v[108:111], a[204:207], v[28:31], v[108:111]\n"
    "v_mul_f32 v127, v127, v162\n"
    "v_cvt_pk_bf16_f32 v120, v120, v121\n"
    "v_mfma_f32_16x16x32_bf16 v[116:119], a[204:207], v[32:35], v[116:119]\n"
    "buffer_load_dwordx4 v243, s[56:59], s66 offen offset:3072 lds\n"
    "v_cvt_pk_bf16_f32 v121, v122, v123\n"
    "s_waitcnt lgkmcnt(4)\n"
    "v_mfma_f32_16x16x32_bf16 v[96:99], a[208:211], v[48:51], v[96:99]\n"
    "v_cvt_pk_bf16_f32 v122, v124, v125\n"
    "s_add_u32 m0, s65, 36864\n"
    "v_mfma_f32_16x16x32_bf16 v[104:107], a[208:211], v[52:55], v[104:107]\n"
    "v_cvt_pk_bf16_f32 v123, v126, v127\n"
    "ds_read_b64_tr_b16 a[192:193], v189 offset:0\n"
    "v_mfma_f32_16x16x32_bf16 v[112:115], a[208:211], v[56:59], v[112:115]\n"
    "ds_read_b64_tr_b16 a[194:195], v189 offset:2048\n"
    "buffer_load_dwordx4 v244, s[56:59], s66 offen offset:0 lds\n"
    "s_nop 0\n"
    "v_mfma_f32_16x16x32_bf16 v[100:103], a[212:215], v[48:51], v[100:103]\n"
    "ds_read_b64_tr_b16 a[196:197], v191 offset:0\n"
    "ds_read_b64_tr_b16 a[198:199], v191 offset:2048\n"
    "v_permlane16_swap_b32 v120, v122\n"
    "v_mfma_f32_16x16x32_bf16 v[108:111], a[212:215], v[52:55], v[108:111]\n"
    "v_permlane16_swap_b32 v121, v123\n"
    "buffer_store_dwordx4 v[120:123], v250, s[60:63], s71 offen offset:0 sc1\n"
    "v_mfma_f32_16x16x32_bf16 v[116:119], a[212:215], v[56:59], v[116:119]\n"
    "buffer_load_dwordx4 v245, s[56:59], s66 offen offset:1024 lds\n"
    "v_mul_f32 v128, v128, v163\n"
    "s_waitcnt lgkmcnt(4)\n"
    "v_mfma_f32_16x16x32_bf16 v[96:99], a[216:219], v[72:75], v[96:99]\n"
    "v_mul_f32 v129, v129, v163\n"
    "v_mul_f32 v130, v130, v163\n"
    "v_mfma_f32_16x16x32_bf16 v[104:107], a[216:219], v[76:79], v[104:107]\n"
    "ds_read_b64_tr_b16 a[200:201], v189 offset:4096\n"
    "ds_read_b64_tr_b16 a[202:203], v189 offset:6144\n"
    "v_mfma_f32_16x16x32_bf16 v[112:115], a[216:219], v[80:83], v[112:115]\n"
    "buffer_load_dwordx4 v246, s[56:59], s66 offen offset:2048 lds\n"
    "v_mul_f32 v131, v131, v163\n"
    "v_mfma_f32_16x16x32_bf16 v[100:103], a[220:223], v[72:75], v[100:103]\n"
    "ds_read_b64_tr_b16 a[204:205], v191 offset:4096\n"
    "ds_read_b64_tr_b16 a[206:207], v191 offset:6144\n"
    "v_mul_f32 v132, v132, v163\n"
    "v_mfma_f32_16x16x32_bf16 v[108:111], a[220:223], v[76:79], v[108:111]\n"
    "v_mul_f32 v133, v133, v163\n"
    "v_mul_f32 v134, v134, v163\n"
    "v_mfma_f32_16x16x32_bf16 v[116:119], a[220:223], v[80:83], v[116:119]\n"
    "buffer_load_dwordx4 v247, s[56:59], s66 offen offset:3072 lds\n"
    "v_mul_f32 v135, v135, v163\n"
    "s_waitcnt lgkmcnt(4)\n"
    "v_mfma_f32_16x16x32_bf16 v[96:99], a[192:195], a[96:99], v[96:99]\n"
    "v_cvt_pk_bf16_f32 v128, v128, v129\n"
    "s_add_u32 s66, s66, 128\n"
    "v_mfma_f32_16x16x32_bf16 v[104:107], a[192:195], a[100:103], v[104:107]\n"
    "v_cvt_pk_bf16_f32 v129, v130, v131\n"
    "ds_read_b64_tr_b16 a[208:209], v189 offset:8192\n"
    "v_mfma_f32_16x16x32_bf16 v[112:115], a[192:195], a[104:107], v[112:115]\n"
    "ds_read_b64_tr_b16 a[210:211], v189 offset:10240\n"
    "v_cvt_pk_bf16_f32 v130, v132, v133\n"
    "v_mfma_f32_16x16x32_bf16 v[100:103], a[196:199], a[96:99], v[100:103]\n"
    "ds_read_b64_tr_b16 a[212:213], v191 offset:8192\n"
    "ds_read_b64_tr_b16 a[214:215], v191 offset:10240\n"
    "v_cvt_pk_bf16_f32 v131, v134, v135\n"
    "v_mfma_f32_16x16x32_bf16 v[108:111], a[196:199], a[100:103], v[108:111]\n"
    "s_nop 0\n"
    "v_permlane16_swap_b32 v128, v130\n"
    "v_mfma_f32_16x16x32_bf16 v[116:119], a[196:199], a[104:107], v[116:119]\n"
    "v_permlane16_swap_b32 v129, v131\n"
    "s_waitcnt lgkmcnt(4)\n"
    "v_mfma_f32_16x16x32_bf16 v[96:99], a[200:203], a[108:111], v[96:99]\n"
    "buffer_store_dwordx4 v[128:131], v250, s[60:63], s71 offen offset:1024 sc1\n"
    "v_mul_f32 v136, v136, v164\n"
    "v_mfma_f32_16x16x32_bf16 v[104:107], a[200:203], a[112:115], v[104:107]\n"
    "ds_read_b64_tr_b16 a[216:217], v189 offset:12288\n"
    "ds_read_b64_tr_b16 a[218:219], v189 offset:14336\n"
    "v_mfma_f32_16x16x32_bf16 v[112:115], a[200:203], a[116:119], v[112:115]\n"
    "v_mul_f32 v137, v137, v164\n"
    "ds_read_b64_tr_b16 a[220:221], v191 offset:12288\n"
    "v_mfma_f32_16x16x32_bf16 v[100:103], a[204:207], a[108:111], v[100:103]\n"
    "ds_read_b64_tr_b16 a[222:223], v191 offset:14336\n"
    "v_mul_f32 v138, v138, v164\n"
    "v_mfma_f32_16x16x32_bf16 v[108:111], a[204:207], a[112:115], v[108:111]\n"
    "v_mul_f32 v139, v139, v164\n"
    "v_mul_f32 v140, v140, v164\n"
    "v_mfma_f32_16x16x32_bf16 v[116:119], a[204:207], a[116:119], v[116:119]\n"
    "v_mul_f32 v141, v141, v164\n"
    "s_waitcnt lgkmcnt(4)\n"
    "v_mfma_f32_16x16x32_bf16 v[96:99], a[208:211], a[120:123], v[96:99]\n"
    "v_mul_f32 v142, v142, v164\n"
    "v_mul_f32 v143, v143, v164\n"
    "v_mfma_f32_16x16x32_bf16 v[104:107], a[208:211], a[124:127], v[104:107]\n"
    "v_cvt_pk_bf16_f32 v136, v136, v137\n"
    "v_cvt_pk_bf16_f32 v137, v138, v139\n"
    "v_mfma_f32_16x16x32_bf16 v[112:115], a[208:211], a[128:131], v[112:115]\n"
    "v_cvt_pk_bf16_f32 v138, v140, v141\n"
    "v_cvt_pk_bf16_f32 v139, v142, v143\n"
    "v_mfma_f32_16x16x32_bf16 v[100:103], a[212:215], a[120:123], v[100:103]\n"
    "s_nop 0\n"
    "v_permlane16_swap_b32 v136, v138\n"
    "v_mfma_f32_16x16x32_bf16 v[108:111], a[212:215], a[124:127], v[108:111]\n"
    "s_waitcnt vmcnt(12) lgkmcnt(0)\n"
    "s_barrier\n"
    "v_mfma_f32_16x16x32_bf16 v[116:119], a[212:215], a[128:131], v[116:119]\n"
    "ds_read_b64_tr_b16 a[192:193], v184 offset:32768\n"
    "ds_read_b64_tr_b16 a[194:195], v184 offset:34816\n"
    "v_mfma_f32_16x16x32_bf16 v[96:99], a[216:219], a[132:135], v[96:99]\n"
    "ds_read_b64_tr_b16 a[196:197], v186 offset:32768\n"
    "ds_read_b64_tr_b16 a[198:199], v186 offset:34816\n"
    "v_mfma_f32_16x16x32_bf16 v[104:107], a[216:219], a[136:139], v[104:107]\n"
    "v_permlane16_swap_b32 v137, v139\n"
    "ds_read_b64_tr_b16 a[200:201], v184 offset:36864\n"
    "v_mfma_f32_16x16x32_bf16 v[112:115], a[216:219], a[140:143], v[112:115]\n"
    "ds_read_b64_tr_b16 a[202:203], v184 offset:38912\n"
    "buffer_store_dwordx4 v[136:139], v250, s[60:63], s71 offen offset:2048 sc1\n"
    "v_mfma_f32_16x16x32_bf16 v[100:103], a[220:223], a[132:135], v[100:103]\n"
    "ds_read_b64_tr_b16 a[204:205], v186 offset:36864\n"
    "ds_read_b64_tr_b16 a[206:207], v186 offset:38912\n"
    "s_add_u32 s71, s71, 12288\n"
    "v_mfma_f32_16x16x32_bf16 v[108:111], a[220:223], a[136:139], v[108:111]\n"
    "v_mfma_f32_16x16x32_bf16 v[116:119], a[220:223], a[140:143], v[116:119]\n"
    "s_waitcnt lgkmcnt(4)\n"
    "v_mfma_f32_16x16x32_bf16 v[120:123], a[192:195], v[0:3], 0\n"
    "s_add_u32 m0, s65, 65536\n"
    "ds_read_b64_tr_b16 a[208:209], v184 offset:40960\n"
    "v_mfma_f32_16x16x32_bf16 v[128:131], a[192:195], v[4:7], 0\n"
    "ds_read_b64_tr_b16 a[210:211], v184 offset:43008\n"
    "buffer_load_dwordx4 v240, s[56:59], s66 offen offset:0 lds\n"
    "v_mfma_f32_16x16x32_bf16 v[136:139], a[192:195], v[8:11], 0\n"
    "ds_read_b64_tr_b16 a[212:213], v186 offset:40960\n"
    "ds_read_b64_tr_b16 a[214:215], v186 offset:43008\n"
    "v_mfma_f32_16x16x32_bf16 v[124:127], a[196:199], v[0:3], 0\n"
    "v_mul_f32 v96, v96, v162\n"
    "v_mul_f32 v97, v97, v162\n"
    "v_mfma_f32_16x16x32_bf16 v[132:135], a[196:199], v[4:7], 0\n"
    "buffer_load_dwordx4 v241, s[56:59], s66 offen offset:1024 lds\n"
    "v_mul_f32 v98, v98, v162\n"
    "v_mfma_f32_16x16x32_bf16 v[140:143], a[196:199], v[8:11], 0\n"
    "v_mul_f32 v99, v99, v162\n"
    "s_waitcnt lgkmcnt(4)\n"
    "v_mfma_f32_16x16x32_bf16 v[120:123], a[200:203], v[24:27], v[120:123]\n"
    "v_mul_f32 v100, v100, v162\n"
    "ds_read_b64_tr_b16 a[216:217], v184 offset:45056\n"
    "v_mfma_f32_16x16x32_bf16 v[128:131], a[200:203], v[28:31], v[128:131]\n"
    "ds_read_b64_tr_b16 a[218:219], v184 offset:47104\n"
    "buffer_load_dwordx4 v242, s[56:59], s66 offen offset:2048 lds\n"
    "v_mfma_f32_16x16x32_bf16 v[136:139], a[200:203], v[32:35], v[136:139]\n"
    "v_mul_f32 v101, v101, v162\n"
    "ds_read_b64_tr_b16 a[220:221], v186 offset:45056\n"
    "v_mfma_f32_16x16x32_bf16 v[124:127], a[204:207], v[24:27], v[124:127]\n"
    "ds_read_b64_tr_b16 a[222:223], v186 offset:47104\n"
    "v_mul_f32 v102, v102, v162\n"
    "v_mfma_f32_16x16x32_bf16 v[132:135], a[204:207], v[28:31], v[132:135]\n"
    "v_mul_f32 v103, v103, v162\n"
    "v_cvt_pk_bf16_f32 v96, v96, v97\n"
    "v_mfma_f32_16x16x32_bf16 v[140:143], a[204:207], v[32:35], v[140:143]\n"
    "buffer_load_dwordx4 v243, s[56:59], s66 offen offset:3072 lds\n"
    "v_cvt_pk_bf16_f32 v97, v98, v99\n"
    "s_waitcnt lgkmcnt(4)\n"
    "v_mfma_f32_16x16x32_bf16 v[120:123], a[208:211], v[48:51], v[120:123]\n"
    "v_cvt_pk_bf16_f32 v98, v100, v101\n"
    "s_add_u32 m0, s65, 69632\n"
    "v_mfma_f32_16x16x32_bf16 v[128:131], a[208:211], v[52:55], v[128:131]\n"
    "v_cvt_pk_bf16_f32 v99, v102, v103\n"
    "ds_read_b64_tr_b16 a[192:193], v185 offset:32768\n"
    "v_mfma_f32_16x16x32_bf16 v[136:139], a[208:211], v[56:59], v[136:139]\n"
    "ds_read_b64_tr_b16 a[194:195], v185 offset:34816\n"
    "buffer_load_dwordx4 v244, s[56:59], s66 offen offset:0 lds\n"
    "s_nop 0\n"
    "v_mfma_f32_16x16x32_bf16 v[124:127], a[212:215], v[48:51], v[124:127]\n"
    "ds_read_b64_tr_b16 a[196:197], v187 offset:32768\n"
    "ds_read_b64_tr_b16 a[198:199], v187 offset:34816\n"
    "v_permlane16_swap_b32 v96, v98\n"
    "v_mfma_f32_16x16x32_bf16 v[132:135], a[212:215], v[52:55], v[132:135]\n"
    "v_permlane16_swap_b32 v97, v99\n"
    "buffer_store_dwordx4 v[96:99], v250, s[60:63], s71 offen offset:0 sc1\n"
    "v_mfma_f32_16x16x32_bf16 v[140:143], a[212:215], v[56:59], v[140:143]\n"
    "buffer_load_dwordx4 v245, s[56:59], s66 offen offset:1024 lds\n"
    "v_mul_f32 v104, v104, v163\n"
    "s_waitcnt lgkmcnt(4)\n"
    "v_mfma_f32_16x16x32_bf16 v[120:123], a[216:219], v[72:75], v[120:123]\n"
    "v_mul_f32 v105, v105, v163\n"
    "v_mul_f32 v106, v106, v163\n"
    "v_mfma_f32_16x16x32_bf16 v[128:131], a[216:219], v[76:79], v[128:131]\n"
    "ds_read_b64_tr_b16 a[200:201], v185 offset:36864\n"
    "ds_read_b64_tr_b16 a[202:203], v185 offset:38912\n"
    "v_mfma_f32_16x16x32_bf16 v[136:139], a[216:219], v[80:83], v[136:139]\n"
    "buffer_load_dwordx4 v246, s[56:59], s66 offen offset:2048 lds\n"
    "v_mul_f32 v107, v107, v163\n"
    "v_mfma_f32_16x16x32_bf16 v[124:127], a[220:223], v[72:75], v[124:127]\n"
    "ds_read_b64_tr_b16 a[204:205], v187 offset:36864\n"
    "ds_read_b64_tr_b16 a[206:207], v187 offset:38912\n"
    "v_mul_f32 v108, v108, v163\n"
    "v_mfma_f32_16x16x32_bf16 v[132:135], a[220:223], v[76:79], v[132:135]\n"
    "v_mul_f32 v109, v109, v163\n"
    "v_mul_f32 v110, v110, v163\n"
    "v_mfma_f32_16x16x32_bf16 v[140:143], a[220:223], v[80:83], v[140:143]\n"
    "buffer_load_dwordx4 v247, s[56:59], s66 offen offset:3072 lds\n"
    "v_mul_f32 v111, v111, v163\n"
    "s_waitcnt lgkmcnt(4)\n"
    "v_mfma_f32_16x16x32_bf16 v[120:123], a[192:195], a[96:99], v[120:123]\n"
    "v_cvt_pk_bf16_f32 v104, v104, v105\n"
    "s_add_u32 s66, s66, 128\n"
    "v_mfma_f32_16x16x32_bf16 v[128:131], a[192:195], a[100:103], v[128:131]\n"
    "v_cvt_pk_bf16_f32 v105, v106, v107\n"
    "ds_read_b64_tr_b16 a[208:209], v185 offset:40960\n"
    "v_mfma_f32_16x16x32_bf16 v[136:139], a[192:195], a[104:107], v[136:139]\n"
    "ds_read_b64_tr_b16 a[210:211], v185 offset:43008\n"
    "v_cvt_pk_bf16_f32 v106, v108, v109\n"
    "v_mfma_f32_16x16x32_bf16 v[124:127], a[196:199], a[96:99], v[124:127]\n"
    "ds_read_b64_tr_b16 a[212:213], v187 offset:40960\n"
    "ds_read_b64_tr_b16 a[214:215], v187 offset:43008\n"
    "v_cvt_pk_bf16_f32 v107, v110, v111\n"
    "v_mfma_f32_16x16x32_bf16 v[132:135], a[196:199], a[100:103], v[132:135]\n"
    "s_nop 0\n"
    "v_permlane16_swap_b32 v104, v106\n"
    "v_mfma_f32_16x16x32_bf16 v[140:143], a[196:199], a[104:107], v[140:143]\n"
    "v_permlane16_swap_b32 v105, v107\n"
    "s_waitcnt lgkmcnt(4)\n"
    "v_mfma_f32_16x16x32_bf16 v[120:123], a[200:203], a[108:111], v[120:123]\n"
    "buffer_store_dwordx4 v[104:107], v250, s[60:63], s71 offen offset:1024 sc1\n"
    "v_mul_f32 v112, v112, v164\n"
    "v_mfma_f32_16x16x32_bf16 v[128:131], a[200:203], a[112:115], v[128:131]\n"
    "ds_read_b64_tr_b16 a[216:217], v185 offset:45056\n"
    "ds_read_b64_tr_b16 a[218:219], v185 offset:47104\n"
    "v_mfma_f32_16x16x32_bf16 v[136:139], a[200:203], a[116:119], v[136:139]\n"
    "v_mul_f32 v113, v113, v164\n"
    "ds_read_b64_tr_b16 a[220:221], v187 offset:45056\n"
    "v_mfma_f32_16x16x32_bf16 v[124:127], a[204:207], a[108:111], v[124:127]\n"
    "ds_read_b64_tr_b16 a[222:223], v187 offset:47104\n"
    "v_mul_f32 v114, v114, v164\n"
    "v_mfma_f32_16x16x32_bf16 v[132:135], a[204:207], a[112:115], v[132:135]\n"
    "v_mul_f32 v115, v115, v164\n"
    "v_mul_f32 v116, v116, v164\n"
    "v_mfma_f32_16x16x32_bf16 v[140:143], a[204:207], a[116:119], v[140:143]\n"
    "v_mul_f32 v117, v117, v164\n"
    "s_waitcnt lgkmcnt(4)\n"
    "v_mfma_f32_16x16x32_bf16 v[120:123], a[208:211], a[120:123], v[120:123]\n"
    "v_mul_f32 v118, v118, v164\n"
    "v_mul_f32 v119, v119, v164\n"
    "v_mfma_f32_16x16x32_bf16 v[128:131], a[208:211], a[124:127], v[128:131]\n"
    "v_cvt_pk_bf16_f32 v112, v112, v113\n"
    "v_cvt_pk_bf16_f32 v113, v114, v115\n"
    "v_mfma_f32_16x16x32_bf16 v[136:139], a[208:211], a[128:131], v[136:139]\n"
    "v_cvt_pk_bf16_f32 v114, v116, v117\n"
    "v_cvt_pk_bf16_f32 v115, v118, v119\n"
    "v_mfma_f32_16x16x32_bf16 v[124:127], a[212:215], a[120:123], v[124:127]\n"
    "s_nop 0\n"
    "v_permlane16_swap_b32 v112, v114\n"
    "v_mfma_f32_16x16x32_bf16 v[132:135], a[212:215], a[124:127], v[132:135]\n"
    "s_waitcnt vmcnt(12) lgkmcnt(0)\n"
    "s_barrier\n"
    "v_mfma_f32_16x16x32_bf16 v[140:143], a[212:215], a[128:131], v[140:143]\n"
    "ds_read_b64_tr_b16 a[192:193], v180 offset:32768\n"
    "ds_read_b64_tr_b16 a[194:195], v180 offset:34816\n"
    "v_mfma_f32_16x16x32_bf16 v[120:123], a[216:219], a[132:135], v[120:123]\n"
    "ds_read_b64_tr_b16 a[196:197], v182 offset:32768\n"
    "ds_read_b64_tr_b16 a[198:199], v182 offset:34816\n"
    "v_mfma_f32_16x16x32_bf16 v[128:131], a[216:219], a[136:139], v[128:131]\n"
    "v_permlane16_swap_b32 v113, v115\n"
    "ds_read_b64_tr_b16 a[200:201], v180 offset:36864\n"
    "v_mfma_f32_16x16x32_bf16 v[136:139], a[216:219], a[140:143], v[136:139]\n"
    "ds_read_b64_tr_b16 a[202:203], v180 offset:38912\n"
    "buffer_store_dwordx4 v[112:115], v250, s[60:63], s71 offen offset:2048 sc1\n"
    "v_mfma_f32_16x16x32_bf16 v[124:127], a[220:223], a[132:135], v[124:127]\n"
    "ds_read_b64_tr_b16 a[204:205], v182 offset:36864\n"
    "ds_read_b64_tr_b16 a[206:207], v182 offset:38912\n"
    "s_add_u32 s71, s71, 12288\n"
    "v_mfma_f32_16x16x32_bf16 v[132:135], a[220:223], a[136:139], v[132:135]\n"
    "v_mfma_f32_16x16x32_bf16 v[140:143], a[220:223], a[140:143], v[140:143]\n"
    "s_waitcnt lgkmcnt(4)\n"
    "v_mfma_f32_16x16x32_bf16 v[96:99], a[192:195], v[0:3], 0\n"
    "s_add_u32 m0, s65, 0\n"
    "ds_read_b64_tr_b16 a[208:209], v180 offset:40960\n"
    "v_mfma_f32_16x16x32_bf16 v[104:107], a[192:195], v[4:7], 0\n"
    "ds_read_b64_tr_b16 a[210:211], v180 offset:43008\n"
    "buffer_load_dwordx4 v240, s[56:59], s66 offen offset:0 lds\n"
    "v_mfma_f32_16x16x32_bf16 v[112:115], a[192:195], v[8:11], 0\n"
    "ds_read_b64_tr_b16 a[212:213], v182 offset:40960\n"
    "ds_read_b64_tr_b16 a[214:215], v182 offset:43008\n"
    "v_mfma_f32_16x16x32_bf16 v[100:103], a[196:199], v[0:3], 0\n"
    "v_mul_f32 v120, v120, v162\n"
    "v_mul_f32 v121, v121, v162\n"
    "v_mfma_f32_16x16x32_bf16 v[108:111], a[196:199], v[4:7], 0\n"
    "buffer_load_dwordx4 v241, s[56:59], s66 offen offset:1024 lds\n"
    "v_mul_f32 v122, v122, v162\n"
    "v_mfma_f32_16x16x32_bf16 v[116:119], a[196:199], v[8:11], 0\n"
    "v_mul_f32 v123, v123, v162\n"
    "s_waitcnt lgkmcnt(4)\n"
    "v_mfma_f32_16x16x32_bf16 v[96:99], a[200:203], v[24:27], v[96:99]\n"
    "v_mul_f32 v124, v124, v162\n"
    "ds_read_b64_tr_b16 a[216:217], v180 offset:45056\n"
    "v_mfma_f32_16x16x32_bf16 v[104:107], a[200:203], v[28:31], v[104:107]\n"
    "ds_read_b64_tr_b16 a[218:219], v180 offset:47104\n"
    "buffer_load_dwordx4 v242, s[56:59], s66 offen offset:2048 lds\n"
    "v_mfma_f32_16x16x32_bf16 v[112:115], a[200:203], v[32:35], v[112:115]\n"
    "v_mul_f32 v125, v125, v162\n"
    "ds_read_b64_tr_b16 a[220:221], v182 offset:45056\n"
    "v_mfma_f32_16x16x32_bf16 v[100:103], a[204:207], v[24:27], v[100:103]\n"
    "ds_read_b64_tr_b16 a[222:223], v182 offset:47104\n"
    "v_mul_f32 v126, v126, v162\n"
    "v_mfma_f32_16x16x32_bf16 v[108:111], a[204:207], v[28:31], v[108:111]\n"
    "v_mul_f32 v127, v127, v162\n"
    "v_cvt_pk_bf16_f32 v120, v120, v121\n"
    "v_mfma_f32_16x16x32_bf16 v[116:119], a[204:207], v[32:35], v[116:119]\n"
    "buffer_load_dwordx4 v243, s[56:59], s66 offen offset:3072 lds\n"
    "v_cvt_pk_bf16_f32 v121, v122, v123\n"
    "s_waitcnt lgkmcnt(4)\n"
    "v_mfma_f32_16x16x32_bf16 v[96:99], a[208:211], v[48:51], v[96:99]\n"
    "v_cvt_pk_bf16_f32 v122, v124, v125\n"
    "s_add_u32 m0, s65, 4096\n"
    "v_mfma_f32_16x16x32_bf16 v[104:107], a[208:211], v[52:55], v[104:107]\n"
    "v_cvt_pk_bf16_f32 v123, v126, v127\n"
    "ds_read_b64_tr_b16 a[192:193], v181 offset:32768\n"
    "v_mfma_f32_16x16x32_bf16 v[112:115], a[208:211], v[56:59], v[112:115]\n"
    "ds_read_b64_tr_b16 a[194:195], v181 offset:34816\n"
    "buffer_load_dwordx4 v244, s[56:59], s66 offen offset:0 lds\n"
    "s_nop 0\n"
    "v_mfma_f32_16x16x32_bf16 v[100:103], a[212:215], v[48:51], v[100:103]\n"
    "ds_read_b64_tr_b16 a[196:197], v183 offset:32768\n"
    "ds_read_b64_tr_b16 a[198:199], v183 offset:34816\n"
    "v_permlane16_swap_b32 v120, v122\n"
    "v_mfma_f32_16x16x32_bf16 v[108:111], a[212:215], v[52:55], v[108:111]\n"
    "v_permlane16_swap_b32 v121, v123\n"
    "buffer_store_dwordx4 v[120:123], v250, s[60:63], s71 offen offset:0 sc1\n"
    "v_mfma_f32_16x16x32_bf16 v[116:119], a[212:215], v[56:59], v[116:119]\n"
    "buffer_load_dwordx4 v245, s[56:59], s66 offen offset:1024 lds\n"
    "v_mul_f32 v128, v128, v163\n"
    "s_waitcnt lgkmcnt(4)\n"
    "v_mfma_f32_16x16x32_bf16 v[96:99], a[216:219], v[72:75], v[96:99]\n"
    "v_mul_f32 v129, v129, v163\n"
    "v_mul_f32 v130, v130, v163\n"
    "v_mfma_f32_16x16x32_bf16 v[104:107], a[216:219], v[76:79], v[104:107]\n"
    "ds_read_b64_tr_b16 a[200:201], v181 offset:36864\n"
    "ds_read_b64_tr_b16 a[202:203], v181 offset:38912\n"
    "v_mfma_f32_16x16x32_bf16 v[112:115], a[216:219], v[80:83], v[112:115]\n"
    "buffer_load_dwordx4 v246, s[56:59], s66 offen offset:2048 lds\n"
    "v_mul_f32 v131, v131, v163\n"
    "v_mfma_f32_16x16x32_bf16 v[100:103], a[220:223], v[72:75], v[100:103]\n"
    "ds_read_b64_tr_b16 a[204:205], v183 offset:36864\n"
    "ds_read_b64_tr_b16 a[206:207], v183 offset:38912\n"
    "v_mul_f32 v132, v132, v163\n"
    "v_mfma_f32_16x16x32_bf16 v[108:111], a[220:223], v[76:79], v[108:111]\n"
    "v_mul_f32 v133, v133, v163\n"
    "v_mul_f32 v134, v134, v163\n"
    "v_mfma_f32_16x16x32_bf16 v[116:119], a[220:223], v[80:83], v[116:119]\n"
    "buffer_load_dwordx4 v247, s[56:59], s66 offen offset:3072 lds\n"
    "v_mul_f32 v135, v135, v163\n"
    "s_waitcnt lgkmcnt(4)\n"
    "v_mfma_f32_16x16x32_bf16 v[96:99], a[192:195], a[96:99], v[96:99]\n"
    "v_cvt_pk_bf16_f32 v128, v128, v129\n"
    "s_add_u32 s66, s66, 128\n"
    "v_mfma_f32_16x16x32_bf16 v[104:107], a[192:195], a[100:103], v[104:107]\n"
    "v_cvt_pk_bf16_f32 v129, v130, v131\n"
    "ds_read_b64_tr_b16 a[208:209], v181 offset:40960\n"
    "v_mfma_f32_16x16x32_bf16 v[112:115], a[192:195], a[104:107], v[112:115]\n"
    "ds_read_b64_tr_b16 a[210:211], v181 offset:43008\n"
    "v_cvt_pk_bf16_f32 v130, v132, v133\n"
    "v_mfma_f32_16x16x32_bf16 v[100:103], a[196:199], a[96:99], v[100:103]\n"
    "ds_read_b64_tr_b16 a[212:213], v183 offset:40960\n"
    "ds_read_b64_tr_b16 a[214:215], v183 offset:43008\n"
    "v_cvt_pk_bf16_f32 v131, v134, v135\n"
    "v_mfma_f32_16x16x32_bf16 v[108:111], a[196:199], a[100:103], v[108:111]\n"
    "s_nop 0\n"
    "v_permlane16_swap_b32 v128, v130\n"
    "v_mfma_f32_16x16x32_bf16 v[116:119], a[196:199], a[104:107], v[116:119]\n"
    "v_permlane16_swap_b32 v129, v131\n"
    "s_waitcnt lgkmcnt(4)\n"
    "v_mfma_f32_16x16x32_bf16 v[96:99], a[200:203], a[108:111], v[96:99]\n"
    "buffer_store_dwordx4 v[128:131], v250, s[60:63], s71 offen offset:1024 sc1\n"
    "v_mul_f32 v136, v136, v164\n"
    "v_mfma_f32_16x16x32_bf16 v[104:107], a[200:203], a[112:115], v[104:107]\n"
    "ds_read_b64_tr_b16 a[216:217], v181 offset:45056\n"
    "ds_read_b64_tr_b16 a[218:219], v181 offset:47104\n"
    "v_mfma_f32_16x16x32_bf16 v[112:115], a[200:203], a[116:119], v[112:115]\n"
    "v_mul_f32 v137, v137, v164\n"
    "ds_read_b64_tr_b16 a[220:221], v183 offset:45056\n"
    "v_mfma_f32_16x16x32_bf16 v[100:103], a[204:207], a[108:111], v[100:103]\n"
    "ds_read_b64_tr_b16 a[222:223], v183 offset:47104\n"
    "v_mul_f32 v138, v138, v164\n"
    "v_mfma_f32_16x16x32_bf16 v[108:111], a[204:207], a[112:115], v[108:111]\n"
    "v_mul_f32 v139, v139, v164\n"
    "v_mul_f32 v140, v140, v164\n"
    "v_mfma_f32_16x16x32_bf16 v[116:119], a[204:207], a[116:119], v[116:119]\n"
    "v_mul_f32 v141, v141, v164\n"
    "s_waitcnt lgkmcnt(4)\n"
    "v_mfma_f32_16x16x32_bf16 v[96:99], a[208:211], a[120:123], v[96:99]\n"
    "v_mul_f32 v142, v142, v164\n"
    "v_mul_f32 v143, v143, v164\n"
    "v_mfma_f32_16x16x32_bf16 v[104:107], a[208:211], a[124:127], v[104:107]\n"
    "v_cvt_pk_bf16_f32 v136, v136, v137\n"
    "v_cvt_pk_bf16_f32 v137, v138, v139\n"
    "v_mfma_f32_16x16x32_bf16 v[112:115], a[208:211], a[128:131], v[112:115]\n"
    "v_cvt_pk_bf16_f32 v138, v140, v141\n"
    "v_cvt_pk_bf16_f32 v139, v142, v143\n"
    "v_mfma_f32_16x16x32_bf16 v[100:103], a[212:215], a[120:123], v[100:103]\n"
    "s_nop 0\n"
    "v_permlane16_swap_b32 v136, v138\n"
    "v_mfma_f32_16x16x32_bf16 v[108:111], a[212:215], a[124:127], v[108:111]\n"
    "s_waitcnt vmcnt(10) lgkmcnt(0)\n"
    "s_barrier\n"
    "v_mfma_f32_16x16x32_bf16 v[116:119], a[212:215], a[128:131], v[116:119]\n"
    "ds_read_b64_tr_b16 a[192:193], v184 offset:0\n"
    "ds_read_b64_tr_b16 a[194:195], v184 offset:2048\n"
    "v_mfma_f32_16x16x32_bf16 v[96:99], a[216:219], a[132:135], v[96:99]\n"
    "ds_read_b64_tr_b16 a[196:197], v186 offset:0\n"
    "ds_read_b64_tr_b16 a[198:199], v186 offset:2048\n"
    "v_mfma_f32_16x16x32_bf16 v[104:107], a[216:219], a[136:139], v[104:107]\n"
    "v_permlane16_swap_b32 v137, v139\n"
    "ds_read_b64_tr_b16 a[200:201], v184 offset:4096\n"
    "v_mfma_f32_16x16x32_bf16 v[112:115], a[216:219], a[140:143], v[112:115]\n"
    "ds_read_b64_tr_b16 a[202:203], v184 offset:6144\n"
    "buffer_store_dwordx4 v[136:139], v250, s[60:63], s71 offen offset:2048 sc1\n"
    "v_mfma_f32_16x16x32_bf16 v[100:103], a[220:223], a[132:135], v[100:103]\n"
    "s_cmp_lg_u32 s64, 0\n"
    "s_cbranch_scc1 .Lnpa_%=\n"
    "s_mov_b64 exec, 1\n"
    "buffer_store_dwordx2 v[210:211], v209, s[44:47], 0 offen offset:128 sc1\n"
    "s_mov_b64 exec, -1\n"
    ".Lnpa_%=:\n"
    "ds_read_b64_tr_b16 a[204:205], v186 offset:4096\n"
    "ds_read_b64_tr_b16 a[206:207], v186 offset:6144\n"
    "s_add_u32 s71, s71, 12288\n"
    "v_mfma_f32_16x16x32_bf16 v[108:111], a[220:223], a[136:139], v[108:111]\n"
    "v_mfma_f32_16x16x32_bf16 v[116:119], a[220:223], a[140:143], v[116:119]\n"
    "s_waitcnt lgkmcnt(4)\n"
    "v_mfma_f32_16x16x32_bf16 v[120:123], a[192:195], v[0:3], 0\n"
    "ds_read_b64_tr_b16 a[208:209], v184 offset:8192\n"
    "ds_read_b64_tr_b16 a[210:211], v184 offset:10240\n"
    "v_mfma_f32_16x16x32_bf16 v[128:131], a[192:195], v[4:7], 0\n"
    "ds_read_b64_tr_b16 a[212:213], v186 offset:8192\n"
    "ds_read_b64_tr_b16 a[214:215], v186 offset:10240\n"
    "v_mfma_f32_16x16x32_bf16 v[136:139], a[192:195], v[8:11], 0\n"
    "v_mul_f32 v96, v96, v162\n"
    "v_mul_f32 v97, v97, v162\n"
    "v_mfma_f32_16x16x32_bf16 v[124:127], a[196:199], v[0:3], 0\n"
    "v_mul_f32 v98, v98, v162\n"
    "v_mul_f32 v99, v99, v162\n"
    "v_mfma_f32_16x16x32_bf16 v[132:135], a[196:199], v[4:7], 0\n"
    "v_mul_f32 v100, v100, v162\n"
    "ds_read_b64_tr_b16 a[216:217], v184 offset:12288\n"
    "v_mfma_f32_16x16x32_bf16 v[140:143], a[196:199], v[8:11], 0\n"
    "ds_read_b64_tr_b16 a[218:219], v184 offset:14336\n"
    "s_waitcnt lgkmcnt(6)\n"
    "v_mfma_f32_16x16x32_bf16 v[120:123], a[200:203], v[24:27], v[120:123]\n"
    "v_mul_f32 v101, v101, v162\n"
    "ds_read_b64_tr_b16 a[220:221], v186 offset:12288\n"
    "v_mfma_f32_16x16x32_bf16 v[128:131], a[200:203], v[28:31], v[128:131]\n"
    "ds_read_b64_tr_b16 a[222:223], v186 offset:14336\n"
    "v_mul_f32 v102, v102, v162\n"
    "v_mfma_f32_16x16x32_bf16 v[136:139], a[200:203], v[32:35], v[136:139]\n"
    "v_mul_f32 v103, v103, v162\n"
    "v_cvt_pk_bf16_f32 v96, v96, v97\n"
    "v_mfma_f32_16x16x32_bf16 v[124:127], a[204:207], v[24:27], v[124:127]\n"
    "v_cvt_pk_bf16_f32 v97, v98, v99\n"
    "v_cvt_pk_bf16_f32 v98, v100, v101\n"
    "v_mfma_f32_16x16x32_bf16 v[132:135], a[204:207], v[28:31], v[132:135]\n"
    "v_cvt_pk_bf16_f32 v99, v102, v103\n"
    "ds_read_b64_tr_b16 a[192:193], v185 offset:0\n"
    "v_mfma_f32_16x16x32_bf16 v[140:143], a[204:207], v[32:35], v[140:143]\n"
    "ds_read_b64_tr_b16 a[194:195], v185 offset:2048\n"
    "s_waitcnt lgkmcnt(6)\n"
    "v_mfma_f32_16x16x32_bf16 v[120:123], a[208:211], v[48:51], v[120:123]\n"
    "s_nop 0\n"
    "ds_read_b64_tr_b16 a[196:197], v187 offset:0\n"
    "v_mfma_f32_16x16x32_bf16 v[128:131], a[208:211], v[52:55], v[128:131]\n"
    "ds_read_b64_tr_b16 a[198:199], v187 offset:2048\n"
    "v_permlane16_swap_b32 v96, v98\n"
    "v_mfma_f32_16x16x32_bf16 v[136:139], a[208:211], v[56:59], v[136:139]\n"
    "v_permlane16_swap_b32 v97, v99\n"
    "buffer_store_dwordx4 v[96:99], v250, s[60:63], s71 offen offset:0 sc1\n"
    "v_mfma_f32_16x16x32_bf16 v[124:127], a[212:215], v[48:51], v[124:127]\n"
    "v_mul_f32 v104, v104, v163\n"
    "v_mul_f32 v105, v105, v163\n"
    "v_mfma_f32_16x16x32_bf16 v[132:135], a[212:215], v[52:55], v[132:135]\n"
    "v_mul_f32 v106, v106, v163\n"
    "ds_read_b64_tr_b16 a[200:201], v185 offset:4096\n"
    "v_mfma_f32_16x16x32_bf16 v[140:143], a[212:215], v[56:59], v[140:143]\n"
    "ds_read_b64_tr_b16 a[202:203], v185 offset:6144\n"
    "s_waitcnt lgkmcnt(6)\n"
    "v_mfma_f32_16x16x32_bf16 v[120:123], a[216:219], v[72:75], v[120:123]\n"
    "v_mul_f32 v107, v107, v163\n"
    "ds_read_b64_tr_b16 a[204:205], v187 offset:4096\n"
    "v_mfma_f32_16x16x32_bf16 v[128:131], a[216:219], v[76:79], v[128:131]\n"
    "ds_read_b64_tr_b16 a[206:207], v187 offset:6144\n"
    "v_mul_f32 v108, v108, v163\n"
    "v_mfma_f32_16x16x32_bf16 v[136:139], a[216:219], v[80:83], v[136:139]\n"
    "v_mul_f32 v109, v109, v163\n"
    "v_mul_f32 v110, v110, v163\n"
    "v_mfma_f32_16x16x32_bf16 v[124:127], a[220:223], v[72:75], v[124:127]\n"
    "v_mul_f32 v111, v111, v163\n"
    "v_cvt_pk_bf16_f32 v104, v104, v105\n"
    "v_mfma_f32_16x16x32_bf16 v[132:135], a[220:223], v[76:79], v[132:135]\n"
    "v_cvt_pk_bf16_f32 v105, v106, v107\n"
    "ds_read_b64_tr_b16 a[208:209], v185 offset:8192\n"
    "v_mfma_f32_16x16x32_bf16 v[140:143], a[220:223], v[80:83], v[140:143]\n"
    "ds_read_b64_tr_b16 a[210:211], v185 offset:10240\n"
    "s_waitcnt lgkmcnt(6)\n"
    "v_mfma_f32_16x16x32_bf16 v[120:123], a[192:195], a[96:99], v[120:123]\n"
    "v_cvt_pk_bf16_f32 v106, v108, v109\n"
    "ds_read_b64_tr_b16 a[212:213], v187 offset:8192\n"
    "v_mfma_f32_16x16x32_bf16 v[128:131], a[192:195], a[100:103], v[128:131]\n"
    "ds_read_b64_tr_b16 a[214:215], v187 offset:10240\n"
    "v_cvt_pk_bf16_f32 v107, v110, v111\n"
    "v_mfma_f32_16x16x32_bf16 v[136:139], a[192:195], a[104:107], v[136:139]\n"
    "s_nop 0\n"
    "v_permlane16_swap_b32 v104, v106\n"
    "v_mfma_f32_16x16x32_bf16 v[124:127], a[196:199], a[96:99], v[124:127]\n"
    "v_permlane16_swap_b32 v105, v107\n"
    "buffer_store_dwordx4 v[104:107], v250, s[60:63], s71 offen offset:1024 sc1\n"
    "v_mfma_f32_16x16x32_bf16 v[132:135], a[196:199], a[100:103], v[132:135]\n"
    "v_mul_f32 v112, v112, v164\n"
    "ds_read_b64_tr_b16 a[216:217], v185 offset:12288\n"
    "v_mfma_f32_16x16x32_bf16 v[140:143], a[196:199], a[104:107], v[140:143]\n"
    "ds_read_b64_tr_b16 a[218:219], v185 offset:14336\n"
    "s_waitcnt lgkmcnt(6)\n"
    "v_mfma_f32_16x16x32_bf16 v[120:123], a[200:203], a[108:111], v[120:123]\n"
    "v_mul_f32 v113, v113, v164\n"
    "ds_read_b64_tr_b16 a[220:221], v187 offset:12288\n"
    "v_mfma_f32_16x16x32_bf16 v[128:131], a[200:203], a[112:115], v[128:131]\n"
    "ds_read_b64_tr_b16 a[222:223], v187 offset:14336\n"
    "v_mul_f32 v114, v114, v164\n"
    "v_mfma_f32_16x16x32_bf16 v[136:139], a[200:203], a[116:119], v[136:139]\n"
    "v_mul_f32 v115, v115, v164\n"
    "v_mul_f32 v116, v116, v164\n"
    "v_mfma_f32_16x16x32_bf16 v[124:127], a[204:207], a[108:111], v[124:127]\n"
    "v_mul_f32 v117, v117, v164\n"
    "v_mul_f32 v118, v118, v164\n"
    "v_mfma_f32_16x16x32_bf16 v[132:135], a[204:207], a[112:115], v[132:135]\n"
    "v_mul_f32 v119, v119, v164\n"
    "v_cvt_pk_bf16_f32 v112, v112, v113\n"
    "v_mfma_f32_16x16x32_bf16 v[140:143], a[204:207], a[116:119], v[140:143]\n"
    "v_cvt_pk_bf16_f32 v113, v114, v115\n"
    "s_waitcnt lgkmcnt(4)\n"
    "v_mfma_f32_16x16x32_bf16 v[120:123], a[208:211], a[120:123], v[120:123]\n"
    "v_cvt_pk_bf16_f32 v114, v116, v117\n"
    "v_cvt_pk_bf16_f32 v115, v118, v119\n"
    "v_mfma_f32_16x16x32_bf16 v[128:131], a[208:211], a[124:127], v[128:131]\n"
    "s_nop 0\n"
    "v_permlane16_swap_b32 v112, v114\n"
    "v_mfma_f32_16x16x32_bf16 v[136:139], a[208:211], a[128:131], v[136:139]\n"
    "v_permlane16_swap_b32 v113, v115\n"
    "buffer_store_dwordx4 v[112:115], v250, s[60:63], s71 offen offset:2048 sc1\n"
    "v_mfma_f32_16x16x32_bf16 v[124:127], a[212:215], a[120:123], v[124:127]\n"
    "s_add_u32 s71, s71, 12288\n"
    "v_mfma_f32_16x16x32_bf16 v[132:135], a[212:215], a[124:127], v[132:135]\n"
    "s_waitcnt vmcnt(5) lgkmcnt(0)\n"
    "s_barrier\n"
    "v_mfma_f32_16x16x32_bf16 v[140:143], a[212:215], a[128:131], v[140:143]\n"
    "ds_read_b64_tr_b16 a[192:193], v180 offset:0\n"
    "ds_read_b64_tr_b16 a[194:195], v180 offset:2048\n"
    "v_mfma_f32_16x16x32_bf16 v[120:123], a[216:219], a[132:135], v[120:123]\n"
    "ds_read_b64_tr_b16 a[196:197], v182 offset:0\n"
    "ds_read_b64_tr_b16 a[198:199], v182 offset:2048\n"
    "v_mfma_f32_16x16x32_bf16 v[128:131], a[216:219], a[136:139], v[128:131]\n"
    "ds_read_b64_tr_b16 a[200:201], v180 offset:4096\n"
    "ds_read_b64_tr_b16 a[202:203], v180 offset:6144\n"
    "v_mfma_f32_16x16x32_bf16 v[136:139], a[216:219], a[140:143], v[136:139]\n"
    "ds_read_b64_tr_b16 a[204:205], v182 offset:4096\n"
    "ds_read_b64_tr_b16 a[206:207], v182 offset:6144\n"
    "v_mfma_f32_16x16x32_bf16 v[124:127], a[220:223], a[132:135], v[124:127]\n"
    "v_mfma_f32_16x16x32_bf16 v[132:135], a[220:223], a[136:139], v[132:135]\n"
    "v_mfma_f32_16x16x32_bf16 v[140:143], a[220:223], a[140:143], v[140:143]\n"
    "s_waitcnt lgkmcnt(4)\n"
    "v_mfma_f32_16x16x32_bf16 v[96:99], a[192:195], v[0:3], 0\n"
    "ds_read_b64_tr_b16 a[208:209], v180 offset:8192\n"
    "ds_read_b64_tr_b16 a[210:211], v180 offset:10240\n"
    "v_mfma_f32_16x16x32_bf16 v[104:107], a[192:195], v[4:7], 0\n"
    "ds_read_b64_tr_b16 a[212:213], v182 offset:8192\n"
    "ds_read_b64_tr_b16 a[214:215], v182 offset:10240\n"
    "v_mfma_f32_16x16x32_bf16 v[112:115], a[192:195], v[8:11], 0\n"
    "v_mul_f32 v120, v120, v162\n"
    "v_mul_f32 v121, v121, v162\n"
    "v_mfma_f32_16x16x32_bf16 v[100:103], a[196:199], v[0:3], 0\n"
    "v_mul_f32 v122, v122, v162\n"
    "v_mul_f32 v123, v123, v162\n"
    "v_mfma_f32_16x16x32_bf16 v[108:111], a[196:199], v[4:7], 0\n"
    "v_mul_f32 v124, v124, v162\n"
    "ds_read_b64_tr_b16 a[216:217], v180 offset:12288\n"
    "v_mfma_f32_16x16x32_bf16 v[116:119], a[196:199], v[8:11], 0\n"
    "ds_read_b64_tr_b16 a[218:219], v180 offset:14336\n"
    "s_waitcnt lgkmcnt(6)\n"
    "v_mfma_f32_16x16x32_bf16 v[96:99], a[200:203], v[24:27], v[96:99]\n"
    "v_mul_f32 v125, v125, v162\n"
    "ds_read_b64_tr_b16 a[220:221], v182 offset:12288\n"
    "v_mfma_f32_16x16x32_bf16 v[104:107], a[200:203], v[28:31], v[104:107]\n"
    "ds_read_b64_tr_b16 a[222:223], v182 offset:14336\n"
    "v_mul_f32 v126, v126, v162\n"
    "v_mfma_f32_16x16x32_bf16 v[112:115], a[200:203], v[32:35], v[112:115]\n"
    "v_mul_f32 v127, v127, v162\n"
    "v_cvt_pk_bf16_f32 v120, v120, v121\n"
    "v_mfma_f32_16x16x32_bf16 v[100:103], a[204:207], v[24:27], v[100:103]\n"
    "v_cvt_pk_bf16_f32 v121, v122, v123\n"
    "v_cvt_pk_bf16_f32 v122, v124, v125\n"
    "v_mfma_f32_16x16x32_bf16 v[108:111], a[204:207], v[28:31], v[108:111]\n"
    "v_cvt_pk_bf16_f32 v123, v126, v127\n"
    "ds_read_b64_tr_b16 a[192:193], v181 offset:0\n"
    "v_mfma_f32_16x16x32_bf16 v[116:119], a[204:207], v[32:35], v[116:119]\n"
    "ds_read_b64_tr_b16 a[194:195], v181 offset:2048\n"
    "s_waitcnt lgkmcnt(6)\n"
    "v_mfma_f32_16x16x32_bf16 v[96:99], a[208:211], v[48:51], v[96:99]\n"
    "s_nop 0\n"
    "ds_read_b64_tr_b16 a[196:197], v183 offset:0\n"
    "v_mfma_f32_16x16x32_bf16 v[104:107], a[208:211], v[52:55], v[104:107]\n"
    "ds_read_b64_tr_b16 a[198:199], v183 offset:2048\n"
    "v_permlane16_swap_b32 v120, v122\n"
    "v_mfma_f32_16x16x32_bf16 v[112:115], a[208:211], v[56:59], v[112:115]\n"
    "v_permlane16_swap_b32 v121, v123\n"
    "buffer_store_dwordx4 v[120:123], v250, s[60:63], s71 offen offset:0 sc1\n"
    "v_mfma_f32_16x16x32_bf16 v[100:103], a[212:215], v[48:51], v[100:103]\n"
    "v_mul_f32 v128, v128, v163\n"
    "v_mul_f32 v129, v129, v163\n"
    "v_mfma_f32_16x16x32_bf16 v[108:111], a[212:215], v[52:55], v[108:111]\n"
    "v_mul_f32 v130, v130, v163\n"
    "ds_read_b64_tr_b16 a[200:201], v181 offset:4096\n"
    "v_mfma_f32_16x16x32_bf16 v[116:119], a[212:215], v[56:59], v[116:119]\n"
    "ds_read_b64_tr_b16 a[202:203], v181 offset:6144\n"
    "s_waitcnt lgkmcnt(6)\n"
    "v_mfma_f32_16x16x32_bf16 v[96:99], a[216:219], v[72:75], v[96:99]\n"
    "v_mul_f32 v131, v131, v163\n"
    "ds_read_b64_tr_b16 a[204:205], v183 offset:4096\n"
    "v_mfma_f32_16x16x32_bf16 v[104:107], a[216:219], v[76:79], v[104:107]\n"
    "ds_read_b64_tr_b16 a[206:207], v183 offset:6144\n"
    "v_mul_f32 v132, v132, v163\n"
    "v_mfma_f32_16x16x32_bf16 v[112:115], a[216:219], v[80:83], v[112:115]\n"
    "v_mul_f32 v133, v133, v163\n"
    "v_mul_f32 v134, v134, v163\n"
    "v_mfma_f32_16x16x32_bf16 v[100:103], a[220:223], v[72:75], v[100:103]\n"
    "v_mul_f32 v135, v135, v163\n"
    "v_cvt_pk_bf16_f32 v128, v128, v129\n"
    "v_mfma_f32_16x16x32_bf16 v[108:111], a[220:223], v[76:79], v[108:111]\n"
    "v_cvt_pk_bf16_f32 v129, v130, v131\n"
    "ds_read_b64_tr_b16 a[208:209], v181 offset:8192\n"
    "v_mfma_f32_16x16x32_bf16 v[116:119], a[220:223], v[80:83], v[116:119]\n"
    "ds_read_b64_tr_b16 a[210:211], v181 offset:10240\n"
    "s_waitcnt lgkmcnt(6)\n"
    "v_mfma_f32_16x16x32_bf16 v[96:99], a[192:195], a[96:99], v[96:99]\n"
    "v_cvt_pk_bf16_f32 v130, v132, v133\n"
    "ds_read_b64_tr_b16 a[212:213], v183 offset:8192\n"
    "v_mfma_f32_16x16x32_bf16 v[104:107], a[192:195], a[100:103], v[104:107]\n"
    "ds_read_b64_tr_b16 a[214:215], v183 offset:10240\n"
    "v_cvt_pk_bf16_f32 v131, v134, v135\n"
    "v_mfma_f32_16x16x32_bf16 v[112:115], a[192:195], a[104:107], v[112:115]\n"
    "s_nop 0\n"
    "v_permlane16_swap_b32 v128, v130\n"
    "v_mfma_f32_16x16x32_bf16 v[100:103], a[196:199], a[96:99], v[100:103]\n"
    "v_permlane16_swap_b32 v129, v131\n"
    "buffer_store_dwordx4 v[128:131], v250, s[60:63], s71 offen offset:1024 sc1\n"
    "v_mfma_f32_16x16x32_bf16 v[108:111], a[196:199], a[100:103], v[108:111]\n"
    "v_mul_f32 v136, v136, v164\n"
    "ds_read_b64_tr_b16 a[216:217], v181 offset:12288\n"
    "v_mfma_f32_16x16x32_bf16 v[116:119], a[196:199], a[104:107], v[116:119]\n"
    "ds_read_b64_tr_b16 a[218:219], v181 offset:14336\n"
    "s_waitcnt lgkmcnt(6)\n"
    "v_mfma_f32_16x16x32_bf16 v[96:99], a[200:203], a[108:111], v[96:99]\n"
    "v_mul_f32 v137, v137, v164\n"
    "ds_read_b64_tr_b16 a[220:221], v183 offset:12288\n"
    "v_mfma_f32_16x16x32_bf16 v[104:107], a[200:203], a[112:115], v[104:107]\n"
    "ds_read_b64_tr_b16 a[222:223], v183 offset:14336\n"
    "v_mul_f32 v138, v138, v164\n"
    "v_mfma_f32_16x16x32_bf16 v[112:115], a[200:203], a[116:119], v[112:115]\n"
    "v_mul_f32 v139, v139, v164\n"
    "v_mul_f32 v140, v140, v164\n"
    "v_mfma_f32_16x16x32_bf16 v[100:103], a[204:207], a[108:111], v[100:103]\n"
    "v_mul_f32 v141, v141, v164\n"
    "v_mul_f32 v142, v142, v164\n"
    "v_mfma_f32_16x16x32_bf16 v[108:111], a[204:207], a[112:115], v[108:111]\n"
    "v_mul_f32 v143, v143, v164\n"
    "v_cvt_pk_bf16_f32 v136, v136, v137\n"
    "v_mfma_f32_16x16x32_bf16 v[116:119], a[204:207], a[116:119], v[116:119]\n"
    "v_cvt_pk_bf16_f32 v137, v138, v139\n"
    "s_waitcnt lgkmcnt(4)\n"
    "v_mfma_f32_16x16x32_bf16 v[96:99], a[208:211], a[120:123], v[96:99]\n"
    "v_cvt_pk_bf16_f32 v138, v140, v141\n"
    "v_cvt_pk_bf16_f32 v139, v142, v143\n"
    "v_mfma_f32_16x16x32_bf16 v[104:107], a[208:211], a[124:127], v[104:107]\n"
    "s_nop 0\n"
    "v_permlane16_swap_b32 v136, v138\n"
    "v_mfma_f32_16x16x32_bf16 v[112:115], a[208:211], a[128:131], v[112:115]\n"
    "v_permlane16_swap_b32 v137, v139\n"
    "buffer_store_dwordx4 v[136:139], v250, s[60:63], s71 offen offset:2048 sc1\n"
    "v_mfma_f32_16x16x32_bf16 v[100:103], a[212:215], a[120:123], v[100:103]\n"
    "s_add_u32 s71, s71, 12288\n"
    "v_mfma_f32_16x16x32_bf16 v[108:111], a[212:215], a[124:127], v[108:111]\n"
    "v_mfma_f32_16x16x32_bf16 v[116:119], a[212:215], a[128:131], v[116:119]\n"
    "s_waitcnt lgkmcnt(0)\n"
    "v_mfma_f32_16x16x32_bf16 v[96:99], a[216:219], a[132:135], v[96:99]\n"
    "v_mfma_f32_16x16x32_bf16 v[104:107], a[216:219], a[136:139], v[104:107]\n"
    "v_mfma_f32_16x16x32_bf16 v[112:115], a[216:219], a[140:143], v[112:115]\n"
    "v_mfma_f32_16x16x32_bf16 v[100:103], a[220:223], a[132:135], v[100:103]\n"
    "v_mfma_f32_16x16x32_bf16 v[108:111], a[220:223], a[136:139], v[108:111]\n"
    "v_mfma_f32_16x16x32_bf16 v[116:119], a[220:223], a[140:143], v[116:119]\n"
    "s_nop 5\n"
    "v_mul_f32 v96, v96, v162\n"
    "v_mul_f32 v104, v104, v163\n"
    "v_mul_f32 v112, v112, v164\n"
    "v_mul_f32 v97, v97, v162\n"
    "v_mul_f32 v105, v105, v163\n"
    "v_mul_f32 v113, v113, v164\n"
    "v_mul_f32 v98, v98, v162\n"
    "v_mul_f32 v106, v106, v163\n"
    "v_mul_f32 v114, v114, v164\n"
    "v_mul_f32 v99, v99, v162\n"
    "v_mul_f32 v107, v107, v163\n"
    "v_mul_f32 v115, v115, v164\n"
    "v_mul_f32 v100, v100, v162\n"
    "v_mul_f32 v108, v108, v163\n"
    "v_mul_f32 v116, v116, v164\n"
    "v_mul_f32 v101, v101, v162\n"
    "v_mul_f32 v109, v109, v163\n"
    "v_mul_f32 v117, v117, v164\n"
    "v_mul_f32 v102, v102, v162\n"
    "v_mul_f32 v110, v110, v163\n"
    "v_mul_f32 v118, v118, v164\n"
    "v_mul_f32 v103, v103, v162\n"
    "v_mul_f32 v111, v111, v163\n"
    "v_mul_f32 v119, v119, v164\n"
    "v_cvt_pk_bf16_f32 v96, v96, v97\n"
    "v_cvt_pk_bf16_f32 v104, v104, v105\n"
    "v_cvt_pk_bf16_f32 v112, v112, v113\n"
    "v_cvt_pk_bf16_f32 v97, v98, v99\n"
    "v_cvt_pk_bf16_f32 v105, v106, v107\n"
    "v_cvt_pk_bf16_f32 v113, v114, v115\n"
    "v_cvt_pk_bf16_f32 v98, v100, v101\n"
    "v_cvt_pk_bf16_f32 v106, v108, v109\n"
    "v_cvt_pk_bf16_f32 v114, v116, v117\n"
    "v_cvt_pk_bf16_f32 v99, v102, v103\n"
    "v_cvt_pk_bf16_f32 v107, v110, v111\n"
    "v_cvt_pk_bf16_f32 v115, v118, v119\n"
    "v_permlane16_swap_b32 v96, v98\n"
    "v_permlane16_swap_b32 v104, v106\n"
    "v_permlane16_swap_b32 v112, v114\n"
    "v_permlane16_swap_b32 v97, v99\n"
    "v_permlane16_swap_b32 v105, v107\n"
    "v_permlane16_swap_b32 v113, v115\n"
    "buffer_store_dwordx4 v[96:99], v250, s[60:63], s71 offen offset:0 sc1\n"
    "buffer_store_dwordx4 v[104:107], v250, s[60:63], s71 offen offset:1024 sc1\n"
    "buffer_store_dwordx4 v[112:115], v250, s[60:63], s71 offen offset:2048 sc1\n"
    "s_cmp_lg_u32 s64, 0\n"
    "s_cbranch_scc1 .Lwait0_%=\n"
    "s_mov_b64 exec, 0xffff\n"
    "v_lshlrev_b32 v213, 3, v254\n"
    "s_mov_b32 s77, 0x20000\n"
    ".Lpoll0_%=:\n"
    "buffer_load_dwordx2 v[214:215], v213, s[44:47], 0 offen offset:128 sc1\n"
    "s_waitcnt vmcnt(0)\n"
    "v_cmp_ne_u64 vcc, s[98:99], v[214:215]\n"
    "s_cbranch_vccz .Lgot0_%=\n"
    "s_sleep 1\n"
    "s_sub_u32 s77, s77, 1\n"
    "s_cbranch_scc0 .Lpoll0_%=\n"
    ".Lgot0_%=:\n"
    "s_mov_b64 exec, -1\n"
    ".Lwait0_%=:\n"
    "s_waitcnt vmcnt(0)\n"
    "s_barrier\n"
    "s_cmp_lg_u32 s64, 0\n"
    "s_cbranch_scc1 .Lnpb_%=\n"
    "s_mov_b64 exec, 1\n"
    "buffer_store_dwordx2 v[210:211], v209, s[44:47], 0 offen offset:256 sc1\n"
    "s_mov_b64 exec, -1\n"
    ".Lnpb_%=:\n"
    "s_mov_b32 s76, 0\n"
    "buffer_load_dwordx4 a[0:3], v176, s[60:63], s76 offen offset:0 sc1\n"
    "s_add_u32 s76, s76, 86016\n"
    "buffer_load_dwordx4 a[4:7], v176, s[60:63], s76 offen offset:0 sc1\n"
    "s_add_u32 s76, s76, 86016\n"
    "buffer_load_dwordx4 a[8:11], v176, s[60:63], s76 offen offset:0 sc1\n"
    "s_add_u32 s76, s76, 86016\n"
    "buffer_load_dwordx4 a[12:15], v176, s[60:63], s76 offen offset:0 sc1\n"
    "s_add_u32 s76, s76, 86016\n"
    "buffer_load_dwordx4 a[16:19], v176, s[60:63], s76 offen offset:0 sc1\n"
    "s_add_u32 s76, s76, 86016\n"
    "buffer_load_dwordx4 a[20:23], v176, s[60:63], s76 offen offset:0 sc1\n"
    "s_add_u32 s76, s76, 86016\n"
    "buffer_load_dwordx4 a[24:27], v176, s[60:63], s76 offen offset:0 sc1\n"
    "s_add_u32 s76, s76, 86016\n"
    "buffer_load_dwordx4 a[28:31], v176, s[60:63], s76 offen offset:0 sc1\n"
    "s_add_u32 s76, s76, 86016\n"
    "buffer_load_dwordx4 a[32:35], v176, s[60:63], s76 offen offset:0 sc1\n"
    "s_add_u32 s76, s76, 86016\n"
    "buffer_load_dwordx4 a[36:39], v176, s[60:63], s76 offen offset:0 sc1\n"
    "s_add_u32 s76, s76, 86016\n"
    "buffer_load_dwordx4 a[40:43], v176, s[60:63], s76 offen offset:0 sc1\n"
    "s_add_u32 s76, s76, 86016\n"
    "buffer_load_dwordx4 a[44:47], v176, s[60:63], s76 offen offset:0 sc1\n"
    "s_add_u32 s76, s76, 86016\n"
    "buffer_load_dwordx4 a[48:51], v176, s[60:63], s76 offen offset:0 sc1\n"
    "s_add_u32 s76, s76, 86016\n"
    "buffer_load_dwordx4 a[52:55], v176, s[60:63], s76 offen offset:0 sc1\n"
    "s_add_u32 s76, s76, 86016\n"
    "buffer_load_dwordx4 a[56:59], v176, s[60:63], s76 offen offset:0 sc1\n"
    "s_add_u32 s76, s76, 86016\n"
    "buffer_load_dwordx4 a[60:63], v176, s[60:63], s76 offen offset:0 sc1\n"
    ".Ljoin_%=:\n"
    "s_cmp_lg_u32 s64, 0\n"
    "s_cbranch_scc1 .Lwait1_%=\n"
    "s_mov_b64 exec, 0xffff\n"
    "v_lshlrev_b32 v213, 3, v254\n"
    "s_mov_b32 s77, 0x20000\n"
    ".Lpoll1_%=:\n"
    "buffer_load_dwordx2 v[214:215], v213, s[44:47], 0 offen offset:256 sc1\n"
    "s_waitcnt vmcnt(0)\n"
    "v_cmp_ne_u64 vcc, s[98:99], v[214:215]\n"
    "s_cbranch_vccz .Lgot1_%=\n"
    "s_sleep 1\n"
    "s_sub_u32 s77, s77, 1\n"
    "s_cbranch_scc0 .Lpoll1_%=\n"
    ".Lgot1_%=:\n"
    "s_cmp_lg_u32 s37, 0\n"
    "s_cbranch_scc1 .Lnoep_%=\n"
    "s_mov_b64 exec, 1\n"
    "s_add_u32 s96, s96, 1\n"
    "v_mov_b32 v210, s96\n"
    "v_mov_b32 v211, s97\n"
    "v_mov_b32 v209, 0\n"
    "buffer_store_dwordx2 v[210:211], v209, s[44:47], 0 offen sc1\n"
    ".Lnoep_%=:\n"
    "s_mov_b64 exec, -1\n"
    ".Lwait1_%=:\n"
    "s_barrier\n"
    "s_mov_b32 s76, 0\n"
    "buffer_load_dwordx4 v[0:3], v177, s[60:63], s76 offen offset:0 sc1\n"
    "s_add_u32 s76, s76, 86016\n"
    "buffer_load_dwordx4 v[4:7], v177, s[60:63], s76 offen offset:0 sc1\n"
    "s_add_u32 s76, s76, 86016\n"
    "buffer_load_dwordx4 v[8:11], v177, s[60:63], s76 offen offset:0 sc1\n"
    "s_add_u32 s76, s76, 86016\n"
    "buffer_load_dwordx4 v[12:15], v177, s[60:63], s76 offen offset:0 sc1\n"
    "s_add_u32 s76, s76, 86016\n"
    "buffer_load_dwordx4 v[16:19], v177, s[60:63], s76 offen offset:0 sc1\n"
    "s_add_u32 s76, s76, 86016\n"
    "buffer_load_dwordx4 v[20:23], v177, s[60:63], s76 offen offset:0 sc1\n"
    "s_add_u32 s76, s76, 86016\n"
    "buffer_load_dwordx4 v[24:27], v177, s[60:63], s76 offen offset:0 sc1\n"
    "s_add_u32 s76, s76, 86016\n"
    "buffer_load_dwordx4 v[28:31], v177, s[60:63], s76 offen offset:0 sc1\n"
    "s_add_u32 s76, s76, 86016\n"
    "buffer_load_dwordx4 v[32:35], v177, s[60:63], s76 offen offset:0 sc1\n"
    "s_add_u32 s76, s76, 86016\n"
    "buffer_load_dwordx4 v[36:39], v177, s[60:63], s76 offen offset:0 sc1\n"
    "s_add_u32 s76, s76, 86016\n"
    "buffer_load_dwordx4 v[40:43], v177, s[60:63], s76 offen offset:0 sc1\n"
    "s_add_u32 s76, s76, 86016\n"
    "buffer_load_dwordx4 v[44:47], v177, s[60:63], s76 offen offset:0 sc1\n"
    "s_add_u32 s76, s76, 86016\n"
    "buffer_load_dwordx4 v[48:51], v177, s[60:63], s76 offen offset:0 sc1\n"
    "s_add_u32 s76, s76, 86016\n"
    "buffer_load_dwordx4 v[52:55], v177, s[60:63], s76 offen offset:0 sc1\n"
    "s_add_u32 s76, s76, 86016\n"
    "buffer_load_dwordx4 v[56:59], v177, s[60:63], s76 offen offset:0 sc1\n"
    "s_add_u32 s76, s76, 86016\n"
    "buffer_load_dwordx4 v[60:63], v177, s[60:63], s76 offen offset:0 sc1\n"
    "s_waitcnt vmcnt(31)\n"
    "v_mfma_f32_16x16x32_bf16 v[64:67], v[226:229], a[0:3], 0\n"
    "v_mfma_f32_16x16x32_bf16 v[68:71], v[224:227], a[0:3], 0\n"
    "s_waitcnt vmcnt(30)\n"
    "v_mfma_f32_16x16x32_bf16 v[64:67], v[226:229], a[4:7], v[64:67]\n"
    "v_mfma_f32_16x16x32_bf16 v[68:71], v[224:227], a[4:7], v[68:71]\n"
    "s_waitcnt vmcnt(29)\n"
    "v_mfma_f32_16x16x32_bf16 v[64:67], v[226:229], a[8:11], v[64:67]\n"
    "v_mfma_f32_16x16x32_bf16 v[68:71], v[224:227], a[8:11], v[68:71]\n"
    "s_waitcnt vmcnt(28)\n"
    "v_mfma_f32_16x16x32_bf16 v[64:67], v[226:229], a[12:15], v[64:67]\n"
    "v_mfma_f32_16x16x32_bf16 v[68:71], v[224:227], a[12:15], v[68:71]\n"
    "s_waitcnt vmcnt(27)\n"
    "v_mfma_f32_16x16x32_bf16 v[64:67], v[226:229], a[16:19], v[64:67]\n"
    "v_mfma_f32_16x16x32_bf16 v[68:71], v[224:227], a[16:19], v[68:71]\n"
    "s_waitcnt vmcnt(26)\n"
    "v_mfma_f32_16x16x32_bf16 v[64:67], v[226:229], a[20:23], v[64:67]\n"
    "v_mfma_f32_16x16x32_bf16 v[68:71], v[224:227], a[20:23], v[68:71]\n"
    "s_waitcnt vmcnt(25)\n"
    "v_mfma_f32_16x16x32_bf16 v[64:67], v[226:229], a[24:27], v[64:67]\n"
    "v_mfma_f32_16x16x32_bf16 v[68:71], v[224:227], a[24:27], v[68:71]\n"
    "s_waitcnt vmcnt(24)\n"
    "v_mfma_f32_16x16x32_bf16 v[64:67], v[226:229], a[28:31], v[64:67]\n"
    "v_mfma_f32_16x16x32_bf16 v[68:71], v[224:227], a[28:31], v[68:71]\n"
    "s_waitcnt vmcnt(23)\n"
    "v_mfma_f32_16x16x32_bf16 v[64:67], v[226:229], a[32:35], v[64:67]\n"
    "v_mfma_f32_16x16x32_bf16 v[68:71], v[224:227], a[32:35], v[68:71]\n"
    "s_waitcnt vmcnt(22)\n"
    "v_mfma_f32_16x16x32_bf16 v[64:67], v[226:229], a[36:39], v[64:67]\n"
    "v_mfma_f32_16x16x32_bf16 v[68:71], v[224:227], a[36:39], v[68:71]\n"
    "s_waitcnt vmcnt(21)\n"
    "v_mfma_f32_16x16x32_bf16 v[64:67], v[226:229], a[40:43], v[64:67]\n"
    "v_mfma_f32_16x16x32_bf16 v[68:71], v[224:227], a[40:43], v[68:71]\n"
    "s_waitcnt vmcnt(20)\n"
    "v_mfma_f32_16x16x32_bf16 v[64:67], v[226:229], a[44:47], v[64:67]\n"
    "v_mfma_f32_16x16x32_bf16 v[68:71], v[224:227], a[44:47], v[68:71]\n"
    "s_waitcnt vmcnt(19)\n"
    "v_mfma_f32_16x16x32_bf16 v[64:67], v[226:229], a[48:51], v[64:67]\n"
    "v_mfma_f32_16x16x32_bf16 v[68:71], v[224:227], a[48:51], v[68:71]\n"
    "s_waitcnt vmcnt(18)\n"
    "v_mfma_f32_16x16x32_bf16 v[64:67], v[226:229], a[52:55], v[64:67]\n"
    "v_mfma_f32_16x16x32_bf16 v[68:71], v[224:227], a[52:55], v[68:71]\n"
    "s_waitcnt vmcnt(17)\n"
    "v_mfma_f32_16x16x32_bf16 v[64:67], v[226:229], a[56:59], v[64:67]\n"
    "v_mfma_f32_16x16x32_bf16 v[68:71], v[224:227], a[56:59], v[68:71]\n"
    "s_waitcnt vmcnt(16)\n"
    "v_mfma_f32_16x16x32_bf16 v[64:67], v[226:229], a[60:63], v[64:67]\n"
    "v_mfma_f32_16x16x32_bf16 v[68:71], v[224:227], a[60:63], v[68:71]\n"
    "s_nop 7\n"
    "v_cvt_pk_bf16_f32 v64, v64, v65\n"
    "v_cvt_pk_bf16_f32 v65, v66, v67\n"
    "v_cvt_pk_bf16_f32 v66, v68, v69\n"
    "v_cvt_pk_bf16_f32 v67, v70, v71\n"
    "buffer_store_dwordx4 v[64:67], v178, s[40:43], 0 offen offset:0 nt\n"
    "s_waitcnt vmcnt(16)\n"
    "v_mfma_f32_16x16x32_bf16 v[72:75], v[226:229], v[0:3], 0\n"
    "v_mfma_f32_16x16x32_bf16 v[76:79], v[224:227], v[0:3], 0\n"
    "s_waitcnt vmcnt(15)\n"
    "v_mfma_f32_16x16x32_bf16 v[72:75], v[226:229], v[4:7], v[72:75]\n"
    "v_mfma_f32_16x16x32_bf16 v[76:79], v[224:227], v[4:7], v[76:79]\n"
    "s_waitcnt vmcnt(14)\n"
    "v_mfma_f32_16x16x32_bf16 v[72:75], v[226:229], v[8:11], v[72:75]\n"
    "v_mfma_f32_16x16x32_bf16 v[76:79], v[224:227], v[8:11], v[76:79]\n"
    "s_waitcnt vmcnt(13)\n"
    "v_mfma_f32_16x16x32_bf16 v[72:75], v[226:229], v[12:15], v[72:75]\n"
    "v_mfma_f32_16x16x32_bf16 v[76:79], v[224:227], v[12:15], v[76:79]\n"
    "s_waitcnt vmcnt(12)\n"
    "v_mfma_f32_16x16x32_bf16 v[72:75], v[226:229], v[16:19], v[72:75]\n"
    "v_mfma_f32_16x16x32_bf16 v[76:79], v[224:227], v[16:19], v[76:79]\n"
    "s_waitcnt vmcnt(11)\n"
    "v_mfma_f32_16x16x32_bf16 v[72:75], v[226:229], v[20:23], v[72:75]\n"
    "v_mfma_f32_16x16x32_bf16 v[76:79], v[224:227], v[20:23], v[76:79]\n"
    "s_waitcnt vmcnt(10)\n"
    "v_mfma_f32_16x16x32_bf16 v[72:75], v[226:229], v[24:27], v[72:75]\n"
    "v_mfma_f32_16x16x32_bf16 v[76:79], v[224:227], v[24:27], v[76:79]\n"
    "s_waitcnt vmcnt(9)\n"
    "v_mfma_f32_16x16x32_bf16 v[72:75], v[226:229], v[28:31], v[72:75]\n"
    "v_mfma_f32_16x16x32_bf16 v[76:79], v[224:227], v[28:31], v[76:79]\n"
    "s_waitcnt vmcnt(8)\n"
    "v_mfma_f32_16x16x32_bf16 v[72:75], v[226:229], v[32:35], v[72:75]\n"
    "v_mfma_f32_16x16x32_bf16 v[76:79], v[224:227], v[32:35], v[76:79]\n"
    "s_waitcnt vmcnt(7)\n"
    "v_mfma_f32_16x16x32_bf16 v[72:75], v[226:229], v[36:39], v[72:75]\n"
    "v_mfma_f32_16x16x32_bf16 v[76:79], v[224:227], v[36:39], v[76:79]\n"
    "s_waitcnt vmcnt(6)\n"
    "v_mfma_f32_16x16x32_bf16 v[72:75], v[226:229], v[40:43], v[72:75]\n"
    "v_mfma_f32_16x16x32_bf16 v[76:79], v[224:227], v[40:43], v[76:79]\n"
    "s_waitcnt vmcnt(5)\n"
    "v_mfma_f32_16x16x32_bf16 v[72:75], v[226:229], v[44:47], v[72:75]\n"
    "v_mfma_f32_16x16x32_bf16 v[76:79], v[224:227], v[44:47], v[76:79]\n"
    "s_waitcnt vmcnt(4)\n"
    "v_mfma_f32_16x16x32_bf16 v[72:75], v[226:229], v[48:51], v[72:75]\n"
    "v_mfma_f32_16x16x32_bf16 v[76:79], v[224:227], v[48:51], v[76:79]\n"
    "s_waitcnt vmcnt(3)\n"
    "v_mfma_f32_16x16x32_bf16 v[72:75], v[226:229], v[52:55], v[72:75]\n"
    "v_mfma_f32_16x16x32_bf16 v[76:79], v[224:227], v[52:55], v[76:79]\n"
    "s_waitcnt vmcnt(2)\n"
    "v_mfma_f32_16x16x32_bf16 v[72:75], v[226:229], v[56:59], v[72:75]\n"
    "v_mfma_f32_16x16x32_bf16 v[76:79], v[224:227], v[56:59], v[76:79]\n"
    "s_waitcnt vmcnt(1)\n"
    "v_mfma_f32_16x16x32_bf16 v[72:75], v[226:229], v[60:63], v[72:75]\n"
    "v_mfma_f32_16x16x32_bf16 v[76:79], v[224:227], v[60:63], v[76:79]\n"
    "s_nop 7\n"
    "v_cvt_pk_bf16_f32 v72, v72, v73\n"
    "v_cvt_pk_bf16_f32 v73, v74, v75\n"
    "v_cvt_pk_bf16_f32 v74, v76, v77\n"
    "v_cvt_pk_bf16_f32 v75, v78, v79\n"
    "buffer_store_dwordx4 v[72:75], v179, s[40:43], 0 offen offset:0 nt\n"
    "s_branch .Lexit_%=\n"
    ".Ltr00_%=:\n"
    "s_call_b64 s[74:75], .Lrare1_%=\n"
    "s_branch .Lbk00_%=\n"
    ".Ltr01_%=:\n"
    "s_call_b64 s[74:75], .Lrare0_%=\n"
    "s_branch .Lbk01_%=\n"
    ".Ltr02_%=:\n"
    "s_call_b64 s[74:75], .Lrare1_%=\n"
    "s_branch .Lbk02_%=\n"
    ".Ltr03_%=:\n"
    "s_call_b64 s[74:75], .Lrare0_%=\n"
    "s_branch .Lbk03_%=\n"
    ".Ltr04_%=:\n"
    "s_call_b64 s[74:75], .Lrare2_%=\n"
    "s_branch .Lbk04_%=\n"
    ".Lfw0_%=:\n"
    "ds_write_b32 v175, v175\n"
    "s_branch .Lfb0_%=\n"
    ".Lfw1_%=:\n"
    "ds_write_b32 v175, v175\n"
    "s_branch .Lfb1_%=\n"
    ".Lfw2_%=:\n"
    "ds_write_b32 v175, v175\n"
    "s_branch .Lfb2_%=\n"
    ".Lfw3_%=:\n"
    "ds_write_b32 v175, v175\n"
    "s_branch .Lfb3_%=\n"
    ".Lfw4_%=:\n"
    "ds_write_b32 v175, v175\n"
    "s_branch .Lfb4_%=\n"
    ".Lrare0_%=:\n"
    "s_nop 7\n"
    "s_nop 1\n"
    "v_max3_f32 v232, v96, v97, v98\n"
    "v_max3_f32 v233, v100, v101, v102\n"
    "v_max3_f32 v234, v104, v105, v106\n"
    "v_max3_f32 v232, v232, v99, v108\n"
    "v_max3_f32 v232, v232, v109, v110\n"
    "v_max_f32 v232, v232, v111\n"
    "v_max3_f32 v233, v233, v103, v112\n"
    "v_max3_f32 v233, v233, v113, v114\n"
    "v_max_f32 v233, v233, v115\n"
    "v_max3_f32 v234, v234, v107, v116\n"
    "v_max3_f32 v234, v234, v117, v118\n"
    "v_max_f32 v234, v234, v119\n"
    "v_mov_b32 v235, v232\n"
    "v_mov_b32 v236, v233\n"
    "v_mov_b32 v237, v234\n"
    "s_nop 1\n"
    "v_permlane16_swap_b32 v232, v235\n"
    "v_permlane16_swap_b32 v233, v236\n"
    "v_permlane16_swap_b32 v234, v237\n"
    "s_nop 0\n"
    "v_max_f32 v232, v232, v235\n"
    "v_max_f32 v233, v233, v236\n"
    "v_max_f32 v234, v234, v237\n"
    "v_mov_b32 v235, v232\n"
    "v_mov_b32 v236, v233\n"
    "v_mov_b32 v237, v234\n"
    "s_nop 1\n"
    "v_permlane32_swap_b32 v232, v235\n"
    "v_permlane32_swap_b32 v233, v236\n"
    "v_permlane32_swap_b32 v234, v237\n"
    "s_nop 0\n"
    "v_max_f32 v232, v232, v235\n"
    "v_max_f32 v233, v233, v236\n"
    "v_max_f32 v234, v234, v237\n"
    "ds_write_b32 v251, v232 offset:0\n"
    "ds_write_b32 v251, v233 offset:64\n"
    "ds_write_b32 v251, v234 offset:128\n"
    "s_waitcnt lgkmcnt(0)\n"
    "s_barrier\n"
    "ds_read_b32 v235, v252 offset:0\n"
    "ds_read_b32 v236, v252 offset:64\n"
    "ds_read_b32 v237, v252 offset:128\n"
    "s_waitcnt lgkmcnt(0)\n"
    "v_max_f32 v232, v232, v235\n"
    "v_max_f32 v233, v233, v236\n"
    "v_max_f32 v234, v234, v237\n"
    "ds_write_b32 v175, v174 offset:0\n"
    "v_max_f32 v232, 0, v232\n"
    "v_max_f32 v233, 0, v233\n"
    "v_max_f32 v234, 0, v234\n"
    "v_exp_f32 v235, -v232\n"
    "v_exp_f32 v236, -v233\n"
    "v_exp_f32 v237, -v234\n"
    "v_sub_f32 v144, v144, v232\n"
    "v_sub_f32 v145, v145, v232\n"
    "v_sub_f32 v146, v146, v232\n"
    "v_sub_f32 v147, v147, v232\n"
    "v_sub_f32 v96, v96, v232\n"
    "v_sub_f32 v97, v97, v232\n"
    "v_sub_f32 v98, v98, v232\n"
    "v_sub_f32 v99, v99, v232\n"
    "v_sub_f32 v108, v108, v232\n"
    "v_sub_f32 v109, v109, v232\n"
    "v_sub_f32 v110, v110, v232\n"
    "v_sub_f32 v111, v111, v232\n"
    "v_sub_f32 v120, v120, v232\n"
    "v_sub_f32 v121, v121, v232\n"
    "v_sub_f32 v122, v122, v232\n"
    "v_sub_f32 v123, v123, v232\n"
    "v_sub_f32 v132, v132, v232\n"
    "v_sub_f32 v133, v133, v232\n"
    "v_sub_f32 v134, v134, v232\n"
    "v_sub_f32 v135, v135, v232\n"
    "v_mul_f32 v212, v212, v235\n"
    "v_mul_f32 v0, v0, v235\n"
    "v_mul_f32 v1, v1, v235\n"
    "v_mul_f32 v2, v2, v235\n"
    "v_mul_f32 v3, v3, v235\n"
    "v_mul_f32 v12, v12, v235\n"
    "v_mul_f32 v13, v13, v235\n"
    "v_mul_f32 v14, v14, v235\n"
    "v_mul_f32 v15, v15, v235\n"
    "v_mul_f32 v24, v24, v235\n"
    "v_mul_f32 v25, v25, v235\n"
    "v_mul_f32 v26, v26, v235\n"
    "v_mul_f32 v27, v27, v235\n"
    "v_mul_f32 v36, v36, v235\n"
    "v_mul_f32 v37, v37, v235\n"
    "v_mul_f32 v38, v38, v235\n"
    "v_mul_f32 v39, v39, v235\n"
    "v_mul_f32 v48, v48, v235\n"
    "v_mul_f32 v49, v49, v235\n"
    "v_mul_f32 v50, v50, v235\n"
    "v_mul_f32 v51, v51, v235\n"
    "v_mul_f32 v60, v60, v235\n"
    "v_mul_f32 v61, v61, v235\n"
    "v_mul_f32 v62, v62, v235\n"
    "v_mul_f32 v63, v63, v235\n"
    "v_mul_f32 v72, v72, v235\n"
    "v_mul_f32 v73, v73, v235\n"
    "v_mul_f32 v74, v74, v235\n"
    "v_mul_f32 v75, v75, v235\n"
    "v_mul_f32 v84, v84, v235\n"
    "v_mul_f32 v85, v85, v235\n"
    "v_mul_f32 v86, v86, v235\n"
    "v_mul_f32 v87, v87, v235\n"
    "v_sub_f32 v148, v148, v233\n"
    "v_sub_f32 v149, v149, v233\n"
    "v_sub_f32 v150, v150, v233\n"
    "v_sub_f32 v151, v151, v233\n"
    "v_sub_f32 v100, v100, v233\n"
    "v_sub_f32 v101, v101, v233\n"
    "v_sub_f32 v102, v102, v233\n"
    "v_sub_f32 v103, v103, v233\n"
    "v_sub_f32 v112, v112, v233\n"
    "v_sub_f32 v113, v113, v233\n"
    "v_sub_f32 v114, v114, v233\n"
    "v_sub_f32 v115, v115, v233\n"
    "v_sub_f32 v124, v124, v233\n"
    "v_sub_f32 v125, v125, v233\n"
    "v_sub_f32 v126, v126, v233\n"
    "v_sub_f32 v127, v127, v233\n"
    "v_sub_f32 v136, v136, v233\n"
    "v_sub_f32 v137, v137, v233\n"
    "v_sub_f32 v138, v138, v233\n"
    "v_sub_f32 v139, v139, v233\n"
    "v_mul_f32 v213, v213, v236\n"
    "v_mul_f32 v4, v4, v236\n"
    "v_mul_f32 v5, v5, v236\n"
    "v_mul_f32 v6, v6, v236\n"
    "v_mul_f32 v7, v7, v236\n"
    "v_mul_f32 v16, v16, v236\n"
    "v_mul_f32 v17, v17, v236\n"
    "v_mul_f32 v18, v18, v236\n"
    "v_mul_f32 v19, v19, v236\n"
    "v_mul_f32 v28, v28, v236\n"
    "v_mul_f32 v29, v29, v236\n"
    "v_mul_f32 v30, v30, v236\n"
    "v_mul_f32 v31, v31, v236\n"
    "v_mul_f32 v40, v40, v236\n"
    "v_mul_f32 v41, v41, v236\n"
    "v_mul_f32 v42, v42, v236\n"
    "v_mul_f32 v43, v43, v236\n"
    "v_mul_f32 v52, v52, v236\n"
    "v_mul_f32 v53, v53, v236\n"
    "v_mul_f32 v54, v54, v236\n"
    "v_mul_f32 v55, v55, v236\n"
    "v_mul_f32 v64, v64, v236\n"
    "v_mul_f32 v65, v65, v236\n"
    "v_mul_f32 v66, v66, v236\n"
    "v_mul_f32 v67, v67, v236\n"
    "v_mul_f32 v76, v76, v236\n"
    "v_mul_f32 v77, v77, v236\n"
    "v_mul_f32 v78, v78, v236\n"
    "v_mul_f32 v79, v79, v236\n"
    "v_mul_f32 v88, v88, v236\n"
    "v_mul_f32 v89, v89, v236\n"
    "v_mul_f32 v90, v90, v236\n"
    "v_mul_f32 v91, v91, v236\n"
    "v_sub_f32 v152, v152, v234\n"
    "v_sub_f32 v153, v153, v234\n"
    "v_sub_f32 v154, v154, v234\n"
    "v_sub_f32 v155, v155, v234\n"
    "v_sub_f32 v104, v104, v234\n"
    "v_sub_f32 v105, v105, v234\n"
    "v_sub_f32 v106, v106, v234\n"
    "v_sub_f32 v107, v107, v234\n"
    "v_sub_f32 v116, v116, v234\n"
    "v_sub_f32 v117, v117, v234\n"
    "v_sub_f32 v118, v118, v234\n"
    "v_sub_f32 v119, v119, v234\n"
    "v_sub_f32 v128, v128, v234\n"
    "v_sub_f32 v129, v129, v234\n"
    "v_sub_f32 v130, v130, v234\n"
    "v_sub_f32 v131, v131, v234\n"
    "v_sub_f32 v140, v140, v234\n"
    "v_sub_f32 v141, v141, v234\n"
    "v_sub_f32 v142, v142, v234\n"
    "v_sub_f32 v143, v143, v234\n"
    "v_mul_f32 v214, v214, v237\n"
    "v_mul_f32 v8, v8, v237\n"
    "v_mul_f32 v9, v9, v237\n"
    "v_mul_f32 v10, v10, v237\n"
    "v_mul_f32 v11, v11, v237\n"
    "v_mul_f32 v20, v20, v237\n"
    "v_mul_f32 v21, v21, v237\n"
    "v_mul_f32 v22, v22, v237\n"
    "v_mul_f32 v23, v23, v237\n"
    "v_mul_f32 v32, v32, v237\n"
    "v_mul_f32 v33, v33, v237\n"
    "v_mul_f32 v34, v34, v237\n"
    "v_mul_f32 v35, v35, v237\n"
    "v_mul_f32 v44, v44, v237\n"
    "v_mul_f32 v45, v45, v237\n"
    "v_mul_f32 v46, v46, v237\n"
    "v_mul_f32 v47, v47, v237\n"
    "v_mul_f32 v56, v56, v237\n"
    "v_mul_f32 v57, v57, v237\n"
    "v_mul_f32 v58, v58, v237\n"
    "v_mul_f32 v59, v59, v237\n"
    "v_mul_f32 v68, v68, v237\n"
    "v_mul_f32 v69, v69, v237\n"
    "v_mul_f32 v70, v70, v237\n"
    "v_mul_f32 v71, v71, v237\n"
    "v_mul_f32 v80, v80, v237\n"
    "v_mul_f32 v81, v81, v237\n"
    "v_mul_f32 v82, v82, v237\n"
    "v_mul_f32 v83, v83, v237\n"
    "v_mul_f32 v92, v92, v237\n"
    "v_mul_f32 v93, v93, v237\n"
    "v_mul_f32 v94, v94, v237\n"
    "v_mul_f32 v95, v95, v237\n"
    "v_exp_f32 v180, v96\n"
    "v_exp_f32 v181, v97\n"
    "v_exp_f32 v182, v98\n"
    "v_exp_f32 v183, v99\n"
    "s_nop 0\n"
    "v_cvt_pk_bf16_f32 v156, v180, v181\n"
    "v_add_f32 v215, v180, v181\n"
    "v_cvt_pk_bf16_f32 v157, v182, v183\n"
    "v_add_f32 v215, v215, v182\n"
    "v_add_f32 v215, v215, v183\n"
    "v_exp_f32 v184, v108\n"
    "v_exp_f32 v185, v109\n"
    "v_exp_f32 v186, v110\n"
    "v_exp_f32 v187, v111\n"
    "s_nop 0\n"
    "v_cvt_pk_bf16_f32 v158, v184, v185\n"
    "v_add_f32 v215, v215, v184\n"
    "v_add_f32 v215, v215, v185\n"
    "v_cvt_pk_bf16_f32 v159, v186, v187\n"
    "v_add_f32 v215, v215, v186\n"
    "ds_write_b128 v172, v[156:159] offset:0\n"
    "v_add_f32 v215, v215, v187\n"
    "v_exp_f32 v180, v100\n"
    "v_exp_f32 v181, v101\n"
    "v_exp_f32 v182, v102\n"
    "v_exp_f32 v183, v103\n"
    "s_nop 0\n"
    "v_cvt_pk_bf16_f32 v160, v180, v181\n"
    "v_add_f32 v216, v180, v181\n"
    "v_cvt_pk_bf16_f32 v161, v182, v183\n"
    "v_add_f32 v216, v216, v182\n"
    "v_add_f32 v216, v216, v183\n"
    "v_exp_f32 v184, v112\n"
    "v_exp_f32 v185, v113\n"
    "v_exp_f32 v186, v114\n"
    "v_exp_f32 v187, v115\n"
    "s_nop 0\n"
    "v_cvt_pk_bf16_f32 v162, v184, v185\n"
    "v_add_f32 v216, v216, v184\n"
    "v_add_f32 v216, v216, v185\n"
    "v_cvt_pk_bf16_f32 v163, v186, v187\n"
    "v_add_f32 v216, v216, v186\n"
    "ds_write_b128 v172, v[160:163] offset:1024\n"
    "v_add_f32 v216, v216, v187\n"
    "v_exp_f32 v180, v104\n"
    "v_exp_f32 v181, v105\n"
    "v_exp_f32 v182, v106\n"
    "v_exp_f32 v183, v107\n"
    "s_nop 0\n"
    "v_cvt_pk_bf16_f32 v164, v180, v181\n"
    "v_add_f32 v217, v180, v181\n"
    "v_cvt_pk_bf16_f32 v165, v182, v183\n"
    "v_add_f32 v217, v217, v182\n"
    "v_add_f32 v217, v217, v183\n"
    "v_exp_f32 v184, v116\n"
    "v_exp_f32 v185, v117\n"
    "v_exp_f32 v186, v118\n"
    "v_exp_f32 v187, v119\n"
    "s_nop 0\n"
    "v_cvt_pk_bf16_f32 v166, v184, v185\n"
    "v_add_f32 v217, v217, v184\n"
    "v_add_f32 v217, v217, v185\n"
    "v_cvt_pk_bf16_f32 v167, v186, v187\n"
    "v_add_f32 v217, v217, v186\n"
    "ds_write_b128 v172, v[164:167] offset:2048\n"
    "v_add_f32 v217, v217, v187\n"
    "s_waitcnt lgkmcnt(0)\n"
    "s_barrier\n"
    "ds_read_b128 a[0:3], v173 offset:0\n"
    "ds_read_b128 a[4:7], v173 offset:1024\n"
    "ds_read_b128 a[8:11], v173 offset:2048\n"
    "ds_read_b128 a[12:15], v173 offset:6144\n"
    "ds_read_b128 a[16:19], v173 offset:7168\n"
    "ds_read_b128 a[20:23], v173 offset:8192\n"
    "s_waitcnt lgkmcnt(0)\n"
    "s_nop 1\n"
    "s_setpc_b64 s[74:75]\n"
    ".Lrare1_%=:\n"
    "s_nop 7\n"
    "s_nop 1\n"
    "v_max3_f32 v232, v120, v121, v122\n"
    "v_max3_f32 v233, v124, v125, v126\n"
    "v_max3_f32 v234, v128, v129, v130\n"
    "v_max3_f32 v232, v232, v123, v132\n"
    "v_max3_f32 v232, v232, v133, v134\n"
    "v_max_f32 v232, v232, v135\n"
    "v_max3_f32 v233, v233, v127, v136\n"
    "v_max3_f32 v233, v233, v137, v138\n"
    "v_max_f32 v233, v233, v139\n"
    "v_max3_f32 v234, v234, v131, v140\n"
    "v_max3_f32 v234, v234, v141, v142\n"
    "v_max_f32 v234, v234, v143\n"
    "v_mov_b32 v235, v232\n"
    "v_mov_b32 v236, v233\n"
    "v_mov_b32 v237, v234\n"
    "s_nop 1\n"
    "v_permlane16_swap_b32 v232, v235\n"
    "v_permlane16_swap_b32 v233, v236\n"
    "v_permlane16_swap_b32 v234, v237\n"
    "s_nop 0\n"
    "v_max_f32 v232, v232, v235\n"
    "v_max_f32 v233, v233, v236\n"
    "v_max_f32 v234, v234, v237\n"
    "v_mov_b32 v235, v232\n"
    "v_mov_b32 v236, v233\n"
    "v_mov_b32 v237, v234\n"
    "s_nop 1\n"
    "v_permlane32_swap_b32 v232, v235\n"
    "v_permlane32_swap_b32 v233, v236\n"
    "v_permlane32_swap_b32 v234, v237\n"
    "s_nop 0\n"
    "v_max_f32 v232, v232, v235\n"
    "v_max_f32 v233, v233, v236\n"
    "v_max_f32 v234, v234, v237\n"
    "ds_write_b32 v251, v232 offset:0\n"
    "ds_write_b32 v251, v233 offset:64\n"
    "ds_write_b32 v251, v234 offset:128\n"
    "s_waitcnt lgkmcnt(0)\n"
    "s_barrier\n"
    "ds_read_b32 v235, v252 offset:0\n"
    "ds_read_b32 v236, v252 offset:64\n"
    "ds_read_b32 v237, v252 offset:128\n"
    "s_waitcnt lgkmcnt(0)\n"
    "v_max_f32 v232, v232, v235\n"
    "v_max_f32 v233, v233, v236\n"
    "v_max_f32 v234, v234, v237\n"
    "ds_write_b32 v175, v174 offset:0\n"
    "v_max_f32 v232, 0, v232\n"
    "v_max_f32 v233, 0, v233\n"
    "v_max_f32 v234, 0, v234\n"
    "v_exp_f32 v235, -v232\n"
    "v_exp_f32 v236, -v233\n"
    "v_exp_f32 v237, -v234\n"
    "v_sub_f32 v144, v144, v232\n"
    "v_sub_f32 v145, v145, v232\n"
    "v_sub_f32 v146, v146, v232\n"
    "v_sub_f32 v147, v147, v232\n"
    "v_sub_f32 v120, v120, v232\n"
    "v_sub_f32 v121, v121, v232\n"
    "v_sub_f32 v122, v122, v232\n"
    "v_sub_f32 v123, v123, v232\n"
    "v_sub_f32 v132, v132, v232\n"
    "v_sub_f32 v133, v133, v232\n"
    "v_sub_f32 v134, v134, v232\n"
    "v_sub_f32 v135, v135, v232\n"
    "v_sub_f32 v96, v96, v232\n"
    "v_sub_f32 v97, v97, v232\n"
    "v_sub_f32 v98, v98, v232\n"
    "v_sub_f32 v99, v99, v232\n"
    "v_sub_f32 v108, v108, v232\n"
    "v_sub_f32 v109, v109, v232\n"
    "v_sub_f32 v110, v110, v232\n"
    "v_sub_f32 v111, v111, v232\n"
    "v_mul_f32 v212, v212, v235\n"
    "v_mul_f32 v0, v0, v235\n"
    "v_mul_f32 v1, v1, v235\n"
    "v_mul_f32 v2, v2, v235\n"
    "v_mul_f32 v3, v3, v235\n"
    "v_mul_f32 v12, v12, v235\n"
    "v_mul_f32 v13, v13, v235\n"
    "v_mul_f32 v14, v14, v235\n"
    "v_mul_f32 v15, v15, v235\n"
    "v_mul_f32 v24, v24, v235\n"
    "v_mul_f32 v25, v25, v235\n"
    "v_mul_f32 v26, v26, v235\n"
    "v_mul_f32 v27, v27, v235\n"
    "v_mul_f32 v36, v36, v235\n"
    "v_mul_f32 v37, v37, v235\n"
    "v_mul_f32 v38, v38, v235\n"
    "v_mul_f32 v39, v39, v235\n"
    "v_mul_f32 v48, v48, v235\n"
    "v_mul_f32 v49, v49, v235\n"
    "v_mul_f32 v50, v50, v235\n"
    "v_mul_f32 v51, v51, v235\n"
    "v_mul_f32 v60, v60, v235\n"
    "v_mul_f32 v61, v61, v235\n"
    "v_mul_f32 v62, v62, v235\n"
    "v_mul_f32 v63, v63, v235\n"
    "v_mul_f32 v72, v72, v235\n"
    "v_mul_f32 v73, v73, v235\n"
    "v_mul_f32 v74, v74, v235\n"
    "v_mul_f32 v75, v75, v235\n"
    "v_mul_f32 v84, v84, v235\n"
    "v_mul_f32 v85, v85, v235\n"
    "v_mul_f32 v86, v86, v235\n"
    "v_mul_f32 v87, v87, v235\n"
    "v_sub_f32 v148, v148, v233\n"
    "v_sub_f32 v149, v149, v233\n"
    "v_sub_f32 v150, v150, v233\n"
    "v_sub_f32 v151, v151, v233\n"
    "v_sub_f32 v124, v124, v233\n"
    "v_sub_f32 v125, v125, v233\n"
    "v_sub_f32 v126, v126, v233\n"
    "v_sub_f32 v127, v127, v233\n"
    "v_sub_f32 v136, v136, v233\n"
    "v_sub_f32 v137, v137, v233\n"
    "v_sub_f32 v138, v138, v233\n"
    "v_sub_f32 v139, v139, v233\n"
    "v_sub_f32 v100, v100, v233\n"
    "v_sub_f32 v101, v101, v233\n"
    "v_sub_f32 v102, v102, v233\n"
    "v_sub_f32 v103, v103, v233\n"
    "v_sub_f32 v112, v112, v233\n"
    "v_sub_f32 v113, v113, v233\n"
    "v_sub_f32 v114, v114, v233\n"
    "v_sub_f32 v115, v115, v233\n"
    "v_mul_f32 v213, v213, v236\n"
    "v_mul_f32 v4, v4, v236\n"
    "v_mul_f32 v5, v5, v236\n"
    "v_mul_f32 v6, v6, v236\n"
    "v_mul_f32 v7, v7, v236\n"
    "v_mul_f32 v16, v16, v236\n"
    "v_mul_f32 v17, v17, v236\n"
    "v_mul_f32 v18, v18, v236\n"
    "v_mul_f32 v19, v19, v236\n"
    "v_mul_f32 v28, v28, v236\n"
    "v_mul_f32 v29, v29, v236\n"
    "v_mul_f32 v30, v30, v236\n"
    "v_mul_f32 v31, v31, v236\n"
    "v_mul_f32 v40, v40, v236\n"
    "v_mul_f32 v41, v41, v236\n"
    "v_mul_f32 v42, v42, v236\n"
    "v_mul_f32 v43, v43, v236\n"
    "v_mul_f32 v52, v52, v236\n"
    "v_mul_f32 v53, v53, v236\n"
    "v_mul_f32 v54, v54, v236\n"
    "v_mul_f32 v55, v55, v236\n"
    "v_mul_f32 v64, v64, v236\n"
    "v_mul_f32 v65, v65, v236\n"
    "v_mul_f32 v66, v66, v236\n"
    "v_mul_f32 v67, v67, v236\n"
    "v_mul_f32 v76, v76, v236\n"
    "v_mul_f32 v77, v77, v236\n"
    "v_mul_f32 v78, v78, v236\n"
    "v_mul_f32 v79, v79, v236\n"
    "v_mul_f32 v88, v88, v236\n"
    "v_mul_f32 v89, v89, v236\n"
    "v_mul_f32 v90, v90, v236\n"
    "v_mul_f32 v91, v91, v236\n"
    "v_sub_f32 v152, v152, v234\n"
    "v_sub_f32 v153, v153, v234\n"
    "v_sub_f32 v154, v154, v234\n"
    "v_sub_f32 v155, v155, v234\n"
    "v_sub_f32 v128, v128, v234\n"
    "v_sub_f32 v129, v129, v234\n"
    "v_sub_f32 v130, v130, v234\n"
    "v_sub_f32 v131, v131, v234\n"
    "v_sub_f32 v140, v140, v234\n"
    "v_sub_f32 v141, v141, v234\n"
    "v_sub_f32 v142, v142, v234\n"
    "v_sub_f32 v143, v143, v234\n"
    "v_sub_f32 v104, v104, v234\n"
    "v_sub_f32 v105, v105, v234\n"
    "v_sub_f32 v106, v106, v234\n"
    "v_sub_f32 v107, v107, v234\n"
    "v_sub_f32 v116, v116, v234\n"
    "v_sub_f32 v117, v117, v234\n"
    "v_sub_f32 v118, v118, v234\n"
    "v_sub_f32 v119, v119, v234\n"
    "v_mul_f32 v214, v214, v237\n"
    "v_mul_f32 v8, v8, v237\n"
    "v_mul_f32 v9, v9, v237\n"
    "v_mul_f32 v10, v10, v237\n"
    "v_mul_f32 v11, v11, v237\n"
    "v_mul_f32 v20, v20, v237\n"
    "v_mul_f32 v21, v21, v237\n"
    "v_mul_f32 v22, v22, v237\n"
    "v_mul_f32 v23, v23, v237\n"
    "v_mul_f32 v32, v32, v237\n"
    "v_mul_f32 v33, v33, v237\n"
    "v_mul_f32 v34, v34, v237\n"
    "v_mul_f32 v35, v35, v237\n"
    "v_mul_f32 v44, v44, v237\n"
    "v_mul_f32 v45, v45, v237\n"
    "v_mul_f32 v46, v46, v237\n"
    "v_mul_f32 v47, v47, v237\n"
    "v_mul_f32 v56, v56, v237\n"
    "v_mul_f32 v57, v57, v237\n"
    "v_mul_f32 v58, v58, v237\n"
    "v_mul_f32 v59, v59, v237\n"
    "v_mul_f32 v68, v68, v237\n"
    "v_mul_f32 v69, v69, v237\n"
    "v_mul_f32 v70, v70, v237\n"
    "v_mul_f32 v71, v71, v237\n"
    "v_mul_f32 v80, v80, v237\n"
    "v_mul_f32 v81, v81, v237\n"
    "v_mul_f32 v82, v82, v237\n"
    "v_mul_f32 v83, v83, v237\n"
    "v_mul_f32 v92, v92, v237\n"
    "v_mul_f32 v93, v93, v237\n"
    "v_mul_f32 v94, v94, v237\n"
    "v_mul_f32 v95, v95, v237\n"
    "v_exp_f32 v180, v120\n"
    "v_exp_f32 v181, v121\n"
    "v_exp_f32 v182, v122\n"
    "v_exp_f32 v183, v123\n"
    "s_nop 0\n"
    "v_cvt_pk_bf16_f32 v156, v180, v181\n"
    "v_add_f32 v218, v180, v181\n"
    "v_cvt_pk_bf16_f32 v157, v182, v183\n"
    "v_add_f32 v218, v218, v182\n"
    "v_add_f32 v218, v218, v183\n"
    "v_exp_f32 v184, v132\n"
    "v_exp_f32 v185, v133\n"
    "v_exp_f32 v186, v134\n"
    "v_exp_f32 v187, v135\n"
    "s_nop 0\n"
    "v_cvt_pk_bf16_f32 v158, v184, v185\n"
    "v_add_f32 v218, v218, v184\n"
    "v_add_f32 v218, v218, v185\n"
    "v_cvt_pk_bf16_f32 v159, v186, v187\n"
    "v_add_f32 v218, v218, v186\n"
    "ds_write_b128 v172, v[156:159] offset:12288\n"
    "v_add_f32 v218, v218, v187\n"
    "v_exp_f32 v180, v124\n"
    "v_exp_f32 v181, v125\n"
    "v_exp_f32 v182, v126\n"
    "v_exp_f32 v183, v127\n"
    "s_nop 0\n"
    "v_cvt_pk_bf16_f32 v160, v180, v181\n"
    "v_add_f32 v219, v180, v181\n"
    "v_cvt_pk_bf16_f32 v161, v182, v183\n"
    "v_add_f32 v219, v219, v182\n"
    "v_add_f32 v219, v219, v183\n"
    "v_exp_f32 v184, v136\n"
    "v_exp_f32 v185, v137\n"
    "v_exp_f32 v186, v138\n"
    "v_exp_f32 v187, v139\n"
    "s_nop 0\n"
    "v_cvt_pk_bf16_f32 v162, v184, v185\n"
    "v_add_f32 v219, v219, v184\n"
    "v_add_f32 v219, v219, v185\n"
    "v_cvt_pk_bf16_f32 v163, v186, v187\n"
    "v_add_f32 v219, v219, v186\n"
    "ds_write_b128 v172, v[160:163] offset:13312\n"
    "v_add_f32 v219, v219, v187\n"
    "v_exp_f32 v180, v128\n"
    "v_exp_f32 v181, v129\n"
    "v_exp_f32 v182, v130\n"
    "v_exp_f32 v183, v131\n"
    "s_nop 0\n"
    "v_cvt_pk_bf16_f32 v164, v180, v181\n"
    "v_add_f32 v220, v180, v181\n"
    "v_cvt_pk_bf16_f32 v165, v182, v183\n"
    "v_add_f32 v220, v220, v182\n"
    "v_add_f32 v220, v220, v183\n"
    "v_exp_f32 v184, v140\n"
    "v_exp_f32 v185, v141\n"
    "v_exp_f32 v186, v142\n"
    "v_exp_f32 v187, v143\n"
    "s_nop 0\n"
    "v_cvt_pk_bf16_f32 v166, v184, v185\n"
    "v_add_f32 v220, v220, v184\n"
    "v_add_f32 v220, v220, v185\n"
    "v_cvt_pk_bf16_f32 v167, v186, v187\n"
    "v_add_f32 v220, v220, v186\n"
    "ds_write_b128 v172, v[164:167] offset:14336\n"
    "v_add_f32 v220, v220, v187\n"
    "s_waitcnt lgkmcnt(0)\n"
    "s_barrier\n"
    "ds_read_b128 a[0:3], v173 offset:12288\n"
    "ds_read_b128 a[4:7], v173 offset:13312\n"
    "ds_read_b128 a[8:11], v173 offset:14336\n"
    "ds_read_b128 a[12:15], v173 offset:18432\n"
    "ds_read_b128 a[16:19], v173 offset:19456\n"
    "ds_read_b128 a[20:23], v173 offset:20480\n"
    "s_waitcnt lgkmcnt(0)\n"
    "s_nop 1\n"
    "s_setpc_b64 s[74:75]\n"
    ".Lrare2_%=:\n"
    "s_nop 7\n"
    "s_nop 1\n"
    "v_max3_f32 v232, v120, v121, v122\n"
    "v_max3_f32 v233, v124, v125, v126\n"
    "v_max3_f32 v234, v128, v129, v130\n"
    "v_max3_f32 v232, v232, v123, v132\n"
    "v_max3_f32 v232, v232, v133, v134\n"
    "v_max_f32 v232, v232, v135\n"
    "v_max3_f32 v233, v233, v127, v136\n"
    "v_max3_f32 v233, v233, v137, v138\n"
    "v_max_f32 v233, v233, v139\n"
    "v_max3_f32 v234, v234, v131, v140\n"
    "v_max3_f32 v234, v234, v141, v142\n"
    "v_max_f32 v234, v234, v143\n"
    "v_mov_b32 v235, v232\n"
    "v_mov_b32 v236, v233\n"
    "v_mov_b32 v237, v234\n"
    "s_nop 1\n"
    "v_permlane16_swap_b32 v232, v235\n"
    "v_permlane16_swap_b32 v233, v236\n"
    "v_permlane16_swap_b32 v234, v237\n"
    "s_nop 0\n"
    "v_max_f32 v232, v232, v235\n"
    "v_max_f32 v233, v233, v236\n"
    "v_max_f32 v234, v234, v237\n"
    "v_mov_b32 v235, v232\n"
    "v_mov_b32 v236, v233\n"
    "v_mov_b32 v237, v234\n"
    "s_nop 1\n"
    "v_permlane32_swap_b32 v232, v235\n"
    "v_permlane32_swap_b32 v233, v236\n"
    "v_permlane32_swap_b32 v234, v237\n"
    "s_nop 0\n"
    "v_max_f32 v232, v232, v235\n"
    "v_max_f32 v233, v233, v236\n"
    "v_max_f32 v234, v234, v237\n"
    "ds_write_b32 v251, v232 offset:0\n"
    "ds_write_b32 v251, v233 offset:64\n"
    "ds_write_b32 v251, v234 offset:128\n"
    "s_waitcnt lgkmcnt(0)\n"
    "s_barrier\n"
    "ds_read_b32 v235, v252 offset:0\n"
    "ds_read_b32 v236, v252 offset:64\n"
    "ds_read_b32 v237, v252 offset:128\n"
    "s_waitcnt lgkmcnt(0)\n"
    "v_max_f32 v232, v232, v235\n"
    "v_max_f32 v233, v233, v236\n"
    "v_max_f32 v234, v234, v237\n"
    "ds_write_b32 v175, v174 offset:0\n"
    "v_max_f32 v232, 0, v232\n"
    "v_max_f32 v233, 0, v233\n"
    "v_max_f32 v234, 0, v234\n"
    "v_exp_f32 v235, -v232\n"
    "v_exp_f32 v236, -v233\n"
    "v_exp_f32 v237, -v234\n"
    "v_sub_f32 v144, v144, v232\n"
    "v_sub_f32 v145, v145, v232\n"
    "v_sub_f32 v146, v146, v232\n"
    "v_sub_f32 v147, v147, v232\n"
    "v_sub_f32 v120, v120, v232\n"
    "v_sub_f32 v121, v121, v232\n"
    "v_sub_f32 v122, v122, v232\n"
    "v_sub_f32 v123, v123, v232\n"
    "v_sub_f32 v132, v132, v232\n"
    "v_sub_f32 v133, v133, v232\n"
    "v_sub_f32 v134, v134, v232\n"
    "v_sub_f32 v135, v135, v232\n"
    "v_sub_f32 v96, v96, v232\n"
    "v_sub_f32 v97, v97, v232\n"
    "v_sub_f32 v98, v98, v232\n"
    "v_sub_f32 v99, v99, v232\n"
    "v_sub_f32 v108, v108, v232\n"
    "v_sub_f32 v109, v109, v232\n"
    "v_sub_f32 v110, v110, v232\n"
    "v_sub_f32 v111, v111, v232\n"
    "v_mul_f32 v212, v212, v235\n"
    "v_mul_f32 v0, v0, v235\n"
    "v_mul_f32 v1, v1, v235\n"
    "v_mul_f32 v2, v2, v235\n"
    "v_mul_f32 v3, v3, v235\n"
    "v_mul_f32 v12, v12, v235\n"
    "v_mul_f32 v13, v13, v235\n"
    "v_mul_f32 v14, v14, v235\n"
    "v_mul_f32 v15, v15, v235\n"
    "v_mul_f32 v24, v24, v235\n"
    "v_mul_f32 v25, v25, v235\n"
    "v_mul_f32 v26, v26, v235\n"
    "v_mul_f32 v27, v27, v235\n"
    "v_mul_f32 v36, v36, v235\n"
    "v_mul_f32 v37, v37, v235\n"
    "v_mul_f32 v38, v38, v235\n"
    "v_mul_f32 v39, v39, v235\n"
    "v_mul_f32 v48, v48, v235\n"
    "v_mul_f32 v49, v49, v235\n"
    "v_mul_f32 v50, v50, v235\n"
    "v_mul_f32 v51, v51, v235\n"
    "v_mul_f32 v60, v60, v235\n"
    "v_mul_f32 v61, v61, v235\n"
    "v_mul_f32 v62, v62, v235\n"
    "v_mul_f32 v63, v63, v235\n"
    "v_mul_f32 v72, v72, v235\n"
    "v_mul_f32 v73, v73, v235\n"
    "v_mul_f32 v74, v74, v235\n"
    "v_mul_f32 v75, v75, v235\n"
    "v_mul_f32 v84, v84, v235\n"
    "v_mul_f32 v85, v85, v235\n"
    "v_mul_f32 v86, v86, v235\n"
    "v_mul_f32 v87, v87, v235\n"
    "v_sub_f32 v148, v148, v233\n"
    "v_sub_f32 v149, v149, v233\n"
    "v_sub_f32 v150, v150, v233\n"
    "v_sub_f32 v151, v151, v233\n"
    "v_sub_f32 v124, v124, v233\n"
    "v_sub_f32 v125, v125, v233\n"
    "v_sub_f32 v126, v126, v233\n"
    "v_sub_f32 v127, v127, v233\n"
    "v_sub_f32 v136, v136, v233\n"
    "v_sub_f32 v137, v137, v233\n"
    "v_sub_f32 v138, v138, v233\n"
    "v_sub_f32 v139, v139, v233\n"
    "v_sub_f32 v100, v100, v233\n"
    "v_sub_f32 v101, v101, v233\n"
    "v_sub_f32 v102, v102, v233\n"
    "v_sub_f32 v103, v103, v233\n"
    "v_sub_f32 v112, v112, v233\n"
    "v_sub_f32 v113, v113, v233\n"
    "v_sub_f32 v114, v114, v233\n"
    "v_sub_f32 v115, v115, v233\n"
    "v_mul_f32 v213, v213, v236\n"
    "v_mul_f32 v4, v4, v236\n"
    "v_mul_f32 v5, v5, v236\n"
    "v_mul_f32 v6, v6, v236\n"
    "v_mul_f32 v7, v7, v236\n"
    "v_mul_f32 v16, v16, v236\n"
    "v_mul_f32 v17, v17, v236\n"
    "v_mul_f32 v18, v18, v236\n"
    "v_mul_f32 v19, v19, v236\n"
    "v_mul_f32 v28, v28, v236\n"
    "v_mul_f32 v29, v29, v236\n"
    "v_mul_f32 v30, v30, v236\n"
    "v_mul_f32 v31, v31, v236\n"
    "v_mul_f32 v40, v40, v236\n"
    "v_mul_f32 v41, v41, v236\n"
    "v_mul_f32 v42, v42, v236\n"
    "v_mul_f32 v43, v43, v236\n"
    "v_mul_f32 v52, v52, v236\n"
    "v_mul_f32 v53, v53, v236\n"
    "v_mul_f32 v54, v54, v236\n"
    "v_mul_f32 v55, v55, v236\n"
    "v_mul_f32 v64, v64, v236\n"
    "v_mul_f32 v65, v65, v236\n"
    "v_mul_f32 v66, v66, v236\n"
    "v_mul_f32 v67, v67, v236\n"
    "v_mul_f32 v76, v76, v236\n"
    "v_mul_f32 v77, v77, v236\n"
    "v_mul_f32 v78, v78, v236\n"
    "v_mul_f32 v79, v79, v236\n"
    "v_mul_f32 v88, v88, v236\n"
    "v_mul_f32 v89, v89, v236\n"
    "v_mul_f32 v90, v90, v236\n"
    "v_mul_f32 v91, v91, v236\n"
    "v_sub_f32 v152, v152, v234\n"
    "v_sub_f32 v153, v153, v234\n"
    "v_sub_f32 v154, v154, v234\n"
    "v_sub_f32 v155, v155, v234\n"
    "v_sub_f32 v128, v128, v234\n"
    "v_sub_f32 v129, v129, v234\n"
    "v_sub_f32 v130, v130, v234\n"
    "v_sub_f32 v131, v131, v234\n"
    "v_sub_f32 v140, v140, v234\n"
    "v_sub_f32 v141, v141, v234\n"
    "v_sub_f32 v142, v142, v234\n"
    "v_sub_f32 v143, v143, v234\n"
    "v_sub_f32 v104, v104, v234\n"
    "v_sub_f32 v105, v105, v234\n"
    "v_sub_f32 v106, v106, v234\n"
    "v_sub_f32 v107, v107, v234\n"
    "v_sub_f32 v116, v116, v234\n"
    "v_sub_f32 v117, v117, v234\n"
    "v_sub_f32 v118, v118, v234\n"
    "v_sub_f32 v119, v119, v234\n"
    "v_mul_f32 v214, v214, v237\n"
    "v_mul_f32 v8, v8, v237\n"
    "v_mul_f32 v9, v9, v237\n"
    "v_mul_f32 v10, v10, v237\n"
    "v_mul_f32 v11, v11, v237\n"
    "v_mul_f32 v20, v20, v237\n"
    "v_mul_f32 v21, v21, v237\n"
    "v_mul_f32 v22, v22, v237\n"
    "v_mul_f32 v23, v23, v237\n"
    "v_mul_f32 v32, v32, v237\n"
    "v_mul_f32 v33, v33, v237\n"
    "v_mul_f32 v34, v34, v237\n"
    "v_mul_f32 v35, v35, v237\n"
    "v_mul_f32 v44, v44, v237\n"
    "v_mul_f32 v45, v45, v237\n"
    "v_mul_f32 v46, v46, v237\n"
    "v_mul_f32 v47, v47, v237\n"
    "v_mul_f32 v56, v56, v237\n"
    "v_mul_f32 v57, v57, v237\n"
    "v_mul_f32 v58, v58, v237\n"
    "v_mul_f32 v59, v59, v237\n"
    "v_mul_f32 v68, v68, v237\n"
    "v_mul_f32 v69, v69, v237\n"
    "v_mul_f32 v70, v70, v237\n"
    "v_mul_f32 v71, v71, v237\n"
    "v_mul_f32 v80, v80, v237\n"
    "v_mul_f32 v81, v81, v237\n"
    "v_mul_f32 v82, v82, v237\n"
    "v_mul_f32 v83, v83, v237\n"
    "v_mul_f32 v92, v92, v237\n"
    "v_mul_f32 v93, v93, v237\n"
    "v_mul_f32 v94, v94, v237\n"
    "v_mul_f32 v95, v95, v237\n"
    "v_exp_f32 v180, v120\n"
    "v_exp_f32 v181, v121\n"
    "v_exp_f32 v182, v122\n"
    "v_exp_f32 v183, v123\n"
    "s_nop 0\n"
    "v_cvt_pk_bf16_f32 v156, v180, v181\n"
    "v_add_f32 v218, v180, v181\n"
    "v_cvt_pk_bf16_f32 v157, v182, v183\n"
    "v_add_f32 v218, v218, v182\n"
    "v_add_f32 v218, v218, v183\n"
    "v_exp_f32 v184, v132\n"
    "v_exp_f32 v185, v133\n"
    "v_exp_f32 v186, v134\n"
    "v_exp_f32 v187, v135\n"
    "s_nop 0\n"
    "v_cvt_pk_bf16_f32 v158, v184, v185\n"
    "v_add_f32 v218, v218, v184\n"
    "v_add_f32 v218, v218, v185\n"
    "v_cvt_pk_bf16_f32 v159, v186, v187\n"
    "v_add_f32 v218, v218, v186\n"
    "ds_write_b128 v172, v[156:159] offset:12288\n"
    "v_add_f32 v218, v218, v187\n"
    "v_exp_f32 v180, v124\n"
    "v_exp_f32 v181, v125\n"
    "v_exp_f32 v182, v126\n"
    "v_exp_f32 v183, v127\n"
    "s_nop 0\n"
    "v_cvt_pk_bf16_f32 v160, v180, v181\n"
    "v_add_f32 v219, v180, v181\n"
    "v_cvt_pk_bf16_f32 v161, v182, v183\n"
    "v_add_f32 v219, v219, v182\n"
    "v_add_f32 v219, v219, v183\n"
    "v_exp_f32 v184, v136\n"
    "v_exp_f32 v185, v137\n"
    "v_exp_f32 v186, v138\n"
    "v_exp_f32 v187, v139\n"
    "s_nop 0\n"
    "v_cvt_pk_bf16_f32 v162, v184, v185\n"
    "v_add_f32 v219, v219, v184\n"
    "v_add_f32 v219, v219, v185\n"
    "v_cvt_pk_bf16_f32 v163, v186, v187\n"
    "v_add_f32 v219, v219, v186\n"
    "ds_write_b128 v172, v[160:163] offset:13312\n"
    "v_add_f32 v219, v219, v187\n"
    "v_exp_f32 v180, v128\n"
    "v_exp_f32 v181, v129\n"
    "v_exp_f32 v182, v130\n"
    "v_exp_f32 v183, v131\n"
    "s_nop 0\n"
    "v_cvt_pk_bf16_f32 v164, v180, v181\n"
    "v_add_f32 v220, v180, v181\n"
    "v_cvt_pk_bf16_f32 v165, v182, v183\n"
    "v_add_f32 v220, v220, v182\n"
    "v_add_f32 v220, v220, v183\n"
    "v_exp_f32 v184, v140\n"
    "v_exp_f32 v185, v141\n"
    "v_exp_f32 v186, v142\n"
    "v_exp_f32 v187, v143\n"
    "s_nop 0\n"
    "v_cvt_pk_bf16_f32 v166, v184, v185\n"
    "v_add_f32 v220, v220, v184\n"
    "v_add_f32 v220, v220, v185\n"
    "v_cvt_pk_bf16_f32 v167, v186, v187\n"
    "v_add_f32 v220, v220, v186\n"
    "ds_write_b128 v172, v[164:167] offset:14336\n"
    "v_add_f32 v220, v220, v187\n"
    "s_waitcnt lgkmcnt(0)\n"
    "s_barrier\n"
    "ds_read_b128 a[144:147], v173 offset:12288\n"
    "ds_read_b128 a[148:151], v173 offset:13312\n"
    "ds_read_b128 a[152:155], v173 offset:14336\n"
    "ds_read_b128 a[156:159], v173 offset:18432\n"
    "ds_read_b128 a[160:163], v173 offset:19456\n"
    "ds_read_b128 a[164:167], v173 offset:20480\n"
    "s_waitcnt lgkmcnt(0)\n"
    "s_nop 1\n"
    "s_setpc_b64 s[74:75]\n"
    ".Lexit_%=:\n"
    :
    : [px] "s"(px), [pk] "s"(pk), [pv] "s"(pv), [pwq] "s"(pwq), [wqsz] "s"(wqsz), [pwo] "s"(pwo), [pp] "s"(pp), [pf] "s"(pf), [pout] "s"(pout),
      [wid] "s"(wid), [bidx] "s"(b), [hidx] "s"(h), [rot] "s"(rot)
    : "v0", "v1", "v2", "v3", "v4", "v5", "v6", "v7", "v8", "v9", "v10", "v11", "v12", "v13", "v14", "v15", "v16", "v17", "v18", "v19", "v20", "v21", "v22", "v23", "v24", "v25", "v26", "v27", "v28", "v29", "v30", "v31", "v32", "v33", "v34", "v35", "v36", "v37", "v38", "v39", "v40", "v41", "v42", "v43", "v44", "v45", "v46", "v47", "v48", "v49", "v50", "v51", "v52", "v53", "v54", "v55", "v56", "v57", "v58", "v59", "v60", "v61", "v62", "v63", "v64", "v65", "v66", "v67", "v68", "v69", "v70", "v71", "v72", "v73", "v74", "v75", "v76", "v77", "v78", "v79", "v80", "v81", "v82", "v83", "v84", "v85", "v86", "v87", "v88", "v89", "v90", "v91", "v92", "v93", "v94", "v95", "v96", "v97", "v98", "v99", "v100", "v101", "v102", "v103", "v104", "v105", "v106", "v107", "v108", "v109", "v110", "v111", "v112", "v113", "v114", "v115", "v116", "v117", "v118", "v119", "v120", "v121", "v122", "v123", "v124", "v125", "v126", "v127", "v128", "v129", "v130", "v131", "v132", "v133", "v134", "v135", "v136", "v137", "v138", "v139", "v140", "v141", "v142", "v143", "v144", "v145", "v146", "v147", "v148", "v149", "v150", "v151", "v152", "v153", "v154", "v155", "v156", "v157", "v158", "v159", "v160", "v161", "v162", "v163", "v164", "v165", "v166", "v167", "v168", "v169", "v170", "v171", "v172", "v173", "v174", "v175", "v176", "v177", "v178", "v179", "v180", "v181", "v182", "v183", "v184", "v185", "v186", "v187", "v188", "v189", "v190", "v191", "v192", "v193", "v194", "v195", "v196", "v197", "v198", "v199", "v200", "v201", "v202", "v203", "v204", "v205", "v206", "v207", "v208", "v209", "v210", "v211", "v212", "v213", "v214", "v215", "v216", "v217", "v218", "v219", "v220", "v221", "v222", "v223", "v224", "v225", "v226", "v227", "v228", "v229", "v230", "v231", "v232", "v233", "v234", "v235", "v236", "v237", "v238", "v239", "v240", "v241", "v242", "v243", "v244", "v245", "v246", "v247", "v248", "v249", "v250", "v251", "v252", "v253", "v254", "v255", "a0", "a1", "a2", "a3", "a4", "a5", "a6", "a7", "a8", "a9", "a10", "a11", "a12", "a13", "a14", "a15", "a16", "a17", "a18", "a19", "a20", "a21", "a22", "a23", "a24", "a25", "a26", "a27", "a28", "a29", "a30", "a31", "a32", "a33", "a34", "a35", "a36", "a37", "a38", "a39", "a40", "a41", "a42", "a43", "a44", "a45", "a46", "a47", "a48", "a49", "a50", "a51", "a52", "a53", "a54", "a55", "a56", "a57", "a58", "a59", "a60", "a61", "a62", "a63", "a64", "a65", "a66", "a67", "a68", "a69", "a70", "a71", "a72", "a73", "a74", "a75", "a76", "a77", "a78", "a79", "a80", "a81", "a82", "a83", "a84", "a85", "a86", "a87", "a88", "a89", "a90", "a91", "a92", "a93", "a94", "a95", "a96", "a97", "a98", "a99", "a100", "a101", "a102", "a103", "a104", "a105", "a106", "a107", "a108", "a109", "a110", "a111", "a112", "a113", "a114", "a115", "a116", "a117", "a118", "a119", "a120", "a121", "a122", "a123", "a124", "a125", "a126", "a127", "a128", "a129", "a130", "a131", "a132", "a133", "a134", "a135", "a136", "a137", "a138", "a139", "a140", "a141", "a142", "a143", "a144", "a145", "a146", "a147", "a148", "a149", "a150", "a151", "a152", "a153", "a154", "a155", "a156", "a157", "a158", "a159", "a160", "a161", "a162", "a163", "a164", "a165", "a166", "a167", "a168", "a169", "a170", "a171", "a172", "a173", "a174", "a175", "a176", "a177", "a178", "a179", "a180", "a181", "a182", "a183", "a184", "a185", "a186", "a187", "a188", "a189", "a190", "a191", "a192", "a193", "a194", "a195", "a196", "a197", "a198", "a199", "a200", "a201", "a202", "a203", "a204", "a205", "a206", "a207", "a208", "a209", "a210", "a211", "a212", "a213", "a214", "a215", "a216", "a217", "a218", "a219", "a220", "a221", "a222", "a223", "a224", "a225", "a226", "a227", "a228", "a229", "a230", "a231", "a232", "a233", "a234", "a235", "a236", "a237", "a238", "a239", "a240", "a241", "a242", "a243", "a244", "a245", "a246", "a247", "a248", "a249", "a250", "a251", "a252", "a253", "a254", "a255", "s36", "s37", "s38", "s39", "s40", "s41", "s42", "s43", "s44", "s45", "s46", "s47", "s48", "s49", "s50", "s51", "s52", "s53", "s54", "s55", "s56", "s57", "s58", "s59", "s60", "s61", "s62", "s63", "s64", "s65", "s66", "s67", "s68", "s69", "s70", "s71", "s72", "s73", "s74", "s75", "s76", "s77", "s78", "s79", "s80", "s81", "s82", "s83", "s84", "s85", "s86", "s87", "s88", "s89", "s90", "s91", "s92", "s93", "s94", "s95", "s96", "s97", "s98", "s99", "vcc", "scc", "memory");
}

extern "C" void kernel_launch(void* const* d_in, const int* in_sizes, int n_in, void* d_out, int out_size, void* d_ws, size_t ws_size,
                              hipStream_t stream) {
  (void)in_sizes; (void)n_in; (void)out_size; (void)ws_size;
  (void)hipFuncSetAttribute((const void*)gqa_main, hipFuncAttributeMaxDynamicSharedMemorySize, LDS_BYTES);
  gqa_main<<<256, 256, LDS_BYTES, stream>>>((const char*)d_in[0], (const char*)d_in[1], (const char*)d_in[2], (const char*)d_in[3], (const char*)d_in[4],
                                            (char*)d_ws, (char*)d_out);
}
```
